```python
import torch

def _softmax(x, axis=-1):
    return torch.softmax(x, dim=axis)
def _gelu(x):
    return torch.nn.functional.gelu(x, approximate="tanh")
def _silu(x):
    return torch.nn.functional.silu(x)
def _sigmoid(x):
    return torch.sigmoid(x)
def _layernorm(x, axis=-1, eps=1e-5):
    m = x.mean(dim=axis, keepdim=True)
    v = x.var(dim=axis, keepdim=True, unbiased=False)
    return (x - m) / torch.sqrt(v + eps)
def _scatter_add(tgt, idx, src, axis=0):
    return tgt.clone().index_add_(axis, idx, src)
def _segment_sum(src, seg, n):
    out = torch.zeros((n,) + src.shape[1:], dtype=src.dtype)
    return out.index_add_(0, seg, src)
def _pad(x, pw):
    flat = tuple(v for pair in reversed(pw) for v in pair)
    return torch.nn.functional.pad(x, flat)

kernel_name = 'standalone__gqa_block_cached__sampling'

def setup_inputs():

    _w = (lambda a: (lambda t: t.to(torch.bfloat16) if t.is_floating_point() else t)(torch.as_tensor(a)))
    B, Sq, D = 8, 1, 512
    Hq, Hkv, Sk, Dh = 32, 8, 8192, 128
    assert Hq % Hkv == 0, (Hq, Hkv)
    return {
        "x": _w((lambda *s: torch.randn(*s, dtype=torch.bfloat16) * 0.8)(B, Sq, D)),
        "k_cache": _w((lambda *s: torch.randn(*s, dtype=torch.bfloat16) * 0.8)(B, Hkv, Sk, Dh)),
        "v_cache": _w((lambda *s: torch.randn(*s, dtype=torch.bfloat16) * 0.05)(B, Hkv, Sk, Dh)),
        "wq": _w((lambda *s: torch.randn(*s, dtype=torch.bfloat16) * 0.8)(D, Hq * Dh) * (D ** -0.5)),
        "wo": _w((lambda *s: torch.randn(*s, dtype=torch.bfloat16) * 0.05)(Hq * Dh, D) * ((Hq * Dh) ** -0.5)),
    }

def reference(x, k_cache, v_cache, wq, wo):
    B, Sq, D = x.shape
    Hq, Dh = 32, 128
    Hkv = k_cache.shape[1]
    rep = Hq // Hkv
    q = (lambda x, *d: x.permute(*d))((x @ wq).reshape(B, Sq, Hq, Dh), 0, 2, 1, 3)
    k = (lambda x, n, axis: torch.repeat_interleave(x, n, dim=axis))(k_cache, rep, axis=1)
    v = (lambda x, n, axis: torch.repeat_interleave(x, n, dim=axis))(v_cache, rep, axis=1)
    scores = torch.einsum("bhqd,bhkd->bhqk", q, k) * (Dh ** -0.5)
    p = _softmax(scores, axis=-1)
    o = torch.einsum("bhqk,bhkd->bhqd", p, v)
    return (lambda x, *d: x.permute(*d))(o, 0, 2, 1, 3).reshape(B, Sq, Hq * Dh) @ wo
```

```cpp
#include <hip/hip_runtime.h>
#include <hip/hip_bf16.h>
#include <cstdint>

using bf16 = __hip_bfloat16;
constexpr int B = 8, DM = 512, HQ = 32, HKV = 8, SK = 8192, D = 128, G = 4;
constexpr int NSPLIT = 4, WAVES = 4, NTHR = 256, PAGE = 16;
constexpr int PPS = SK / PAGE / NSPLIT, PPW = PPS / WAVES;
constexpr float SCALE = 0.088388347648318440f;

using bf16x2 = __attribute__((ext_vector_type(2))) __bf16;
using u32x4  = __attribute__((ext_vector_type(4))) unsigned;
using f32x4  = __attribute__((ext_vector_type(4))) float;
__device__ __forceinline__ float dot2(unsigned k, unsigned q, float acc) {
  return __builtin_amdgcn_fdot2_f32_bf16(__builtin_bit_cast(bf16x2, k), __builtin_bit_cast(bf16x2, q), acc, false);
}
__device__ __forceinline__ unsigned cvtpk(float lo, float hi) {
  unsigned r; asm volatile("v_cvt_pk_bf16_f32 %0, %1, %2" : "=v"(r) : "v"(lo), "v"(hi)); return r;
}
template <int CTRL> __device__ __forceinline__ float dpp(float x) {
  return __builtin_bit_cast(float, __builtin_amdgcn_mov_dpp(__builtin_bit_cast(int, x), CTRL, 0xf, 0xf, true));
}
constexpr int XOR1 = 0xB1, XOR2 = 0x4E, XOR7 = 0x141, XOR8 = 0x128, ROR4 = 0x124;
__device__ __forceinline__ float xrow16_max(float x) {
  auto s = __builtin_amdgcn_permlane16_swap(__float_as_uint(x), __float_as_uint(x), false, false);
  x = fmaxf(__uint_as_float(s[0]), __uint_as_float(s[1]));
  auto t = __builtin_amdgcn_permlane32_swap(__float_as_uint(x), __float_as_uint(x), false, false);
  return fmaxf(__uint_as_float(t[0]), __uint_as_float(t[1]));
}
__device__ __forceinline__ float xrow16_sum(float x) {
  auto s = __builtin_amdgcn_permlane16_swap(__float_as_uint(x), __float_as_uint(x), false, false);
  x = __uint_as_float(s[0]) + __uint_as_float(s[1]);
  auto t = __builtin_amdgcn_permlane32_swap(__float_as_uint(x), __float_as_uint(x), false, false);
  return __uint_as_float(t[0]) + __uint_as_float(t[1]);
}
__device__ __forceinline__ float bflo(unsigned u) { return __builtin_bit_cast(float, u << 16); }
__device__ __forceinline__ float bfhi(unsigned u) { return __builtin_bit_cast(float, u & 0xffff0000u); }

constexpr size_t WS_Q = 0;
constexpr size_t WS_PML = 65536;
constexpr size_t WS_PO = WS_PML + 65536;

__global__ void __launch_bounds__(256) qproj(const bf16* __restrict__ x, const bf16* __restrict__ wq, bf16* __restrict__ q) {
  __shared__ float xs[B][DM];
  __shared__ float red[64][B * 32 + 1];
  const int tid = threadIdx.x;
  const int kg = tid >> 2, cg = tid & 3, n0 = blockIdx.x * 32 + cg * 8;
  u32x4 w[8];
#pragma unroll
  for (int i = 0; i < 8; ++i) w[i] = *reinterpret_cast<const u32x4*>(wq + (size_t)(kg + 64 * i) * 4096 + n0);
  for (int i = tid; i < B * DM; i += 256) xs[i / DM][i % DM] = __bfloat162float(x[i]);
  __syncthreads();
  float acc[B][8];
#pragma unroll
  for (int b = 0; b < B; ++b)
#pragma unroll
    for (int j = 0; j < 8; ++j) acc[b][j] = 0.f;
#pragma unroll
  for (int i = 0; i < 8; ++i) {
    float wf[8];
#pragma unroll
    for (int j = 0; j < 4; ++j) { wf[2 * j] = bflo(w[i][j]); wf[2 * j + 1] = bfhi(w[i][j]); }
#pragma unroll
    for (int b = 0; b < B; ++b) { const float xv = xs[b][kg + 64 * i];
#pragma unroll
      for (int j = 0; j < 8; ++j) acc[b][j] = fmaf(xv, wf[j], acc[b][j]); }
  }
#pragma unroll
  for (int b = 0; b < B; ++b)
#pragma unroll
    for (int j = 0; j < 8; ++j) red[kg][b * 32 + cg * 8 + j] = acc[b][j];
  __syncthreads();
  float s = 0.f;
  for (int k = 0; k < 64; ++k) s += red[k][tid];
  q[(tid >> 5) * 4096 + blockIdx.x * 32 + (tid & 31)] = __float2bfloat16(s);
}

constexpr int LDS_P = 0, LDS_MRG = WAVES * 64, MRG_REC = 4 + D, LDS_FLOATS = LDS_MRG + WAVES * 4 * MRG_REC;
__global__ void __launch_bounds__(NTHR) attn_split(const bf16* __restrict__ q, const bf16* __restrict__ kc, const bf16* __restrict__ vc, float* __restrict__ pml, float* __restrict__ po) {
  __shared__ __attribute__((aligned(16))) float lds[LDS_FLOATS];
  const int s = blockIdx.x, kvh = blockIdx.y, b = blockIdx.z;
  const int tid = threadIdx.x, lane = tid & 63, wave = __builtin_amdgcn_readfirstlane(tid >> 6), r = lane >> 4, c = lane & 15;
  constexpr float C2 = SCALE * 1.4426950408889634f;
  const int p0 = s * PPS;
  u32x4 qpk[4];
#pragma unroll
  for (int g = 0; g < 4; ++g) qpk[g] = *reinterpret_cast<const u32x4*>(q + ((size_t)b * HQ + kvh * G + g) * D + 8 * c);
  const size_t loff = (size_t)r * D + 8 * c;
#define ISSUE(i_) do { const size_t pb_ = ((size_t)(b * HKV + kvh) * SK + (size_t)(p0 + wave + 4 * (i_)) * PAGE) * D + loff; \
    _Pragma("unroll") for (int t_ = 0; t_ < 4; ++t_) { Kn[t_] = __builtin_nontemporal_load(reinterpret_cast<const u32x4*>(kc + pb_ + t_ * 4 * D)); Vn[t_] = __builtin_nontemporal_load(reinterpret_cast<const u32x4*>(vc + pb_ + t_ * 4 * D)); } } while (0)
  u32x4 Kn[4], Vn[4];
  float m = -INFINITY, l = 0.f, acc[32];
#pragma unroll
  for (int k = 0; k < 32; ++k) acc[k] = 0.f;
  ISSUE(0);
  for (int i = 0; i < PPW; ++i) {
    u32x4 Kc[4], Vc[4];
#pragma unroll
    for (int t = 0; t < 4; ++t) { Kc[t] = Kn[t]; Vc[t] = Vn[t]; }
    if (i + 1 < PPW) ISSUE(i + 1);
    float x[16];
#pragma unroll
    for (int t = 0; t < 4; ++t)
#pragma unroll
      for (int g = 0; g < 4; ++g) { float d = 0.f;
#pragma unroll
        for (int k = 0; k < 4; ++k) d = dot2(Kc[t][k], qpk[g][k], d);
        x[t * 4 + g] = d; }
#define TR_STEP(HALF, CTRL, BIT) _Pragma("unroll") for (int h = 0; h < (HALF); ++h) { \
      const float lo_ = x[h], hi_ = x[h + (HALF)]; const float t1_ = lo_ + dpp<CTRL>(lo_), t2_ = hi_ + dpp<CTRL>(hi_); \
      x[h] = (c & (BIT)) ? t2_ : t1_; }
    TR_STEP(8, XOR8, 8) TR_STEP(4, XOR7, 4) TR_STEP(2, XOR2, 2) TR_STEP(1, XOR1, 1)
#undef TR_STEP
    const float sc = x[0] * C2;
    float pm = sc; pm = fmaxf(pm, dpp<ROR4>(pm)); pm = fmaxf(pm, dpp<XOR8>(pm)); pm = xrow16_max(pm);
    const float mn = fmaxf(m, pm);
    const float alpha = __builtin_amdgcn_exp2f(m - mn), p = __builtin_amdgcn_exp2f(sc - mn);
    l = fmaf(l, alpha, p); m = mn;
#pragma unroll
    for (int q4 = 0; q4 < 4; ++q4) {
      const float al = q4 == 0 ? dpp<0x00>(alpha) : q4 == 1 ? dpp<0x55>(alpha) : q4 == 2 ? dpp<0xAA>(alpha) : dpp<0xFF>(alpha);
#pragma unroll
      for (int k = 0; k < 8; ++k) acc[q4 * 8 + k] *= al; }
    float* prow = lds + LDS_P + (wave * 4 + r) * 16;
    prow[c] = p;
    asm volatile("s_waitcnt lgkmcnt(0)" ::: "memory");
    f32x4 pr[4];
#pragma unroll
    for (int u = 0; u < 4; ++u) pr[u] = *reinterpret_cast<const f32x4*>(prow + 4 * u);
#pragma unroll
    for (int t = 0; t < 4; ++t) {
      float vf[8];
#pragma unroll
      for (int k = 0; k < 4; ++k) { vf[2 * k] = bflo(Vc[t][k]); vf[2 * k + 1] = bfhi(Vc[t][k]); }
#pragma unroll
      for (int g = 0; g < 4; ++g) { const float pg_ = pr[t][g];
#pragma unroll
        for (int k = 0; k < 8; ++k) acc[g * 8 + k] = fmaf(pg_, vf[k], acc[g * 8 + k]); } }
  }
#undef ISSUE
#pragma unroll
  for (int k = 0; k < 32; ++k) acc[k] = xrow16_sum(acc[k]);
  { float t = l; t += dpp<ROR4>(t); t += dpp<XOR8>(t); l = xrow16_sum(t); }
  float* mrg = lds + LDS_MRG;
  if (r == 0) {
#pragma unroll
    for (int g = 0; g < 4; ++g) {
      float* rec = mrg + (wave * 4 + g) * MRG_REC;
#pragma unroll
      for (int k = 0; k < 8; k += 4) *reinterpret_cast<f32x4*>(rec + 4 + 8 * c + k) = f32x4{acc[g * 8 + k], acc[g * 8 + k + 1], acc[g * 8 + k + 2], acc[g * 8 + k + 3]};
      if (c == g) { rec[0] = m; rec[1] = l; } } }
  __syncthreads();
  {
    const int g = wave, d0 = 2 * lane;
    const size_t row = (size_t)b * HQ + kvh * G + g;
    float M = -INFINITY;
#pragma unroll
    for (int w = 0; w < WAVES; ++w) M = fmaxf(M, mrg[(w * 4 + g) * MRG_REC]);
    float L = 0.f, o0 = 0.f, o1 = 0.f;
#pragma unroll
    for (int w = 0; w < WAVES; ++w) { const float* rec = mrg + (w * 4 + g) * MRG_REC; const float lw = rec[1];
      const float wt = __builtin_amdgcn_exp2f(rec[0] - M);
      L = fmaf(wt, lw, L); o0 = fmaf(wt, rec[4 + d0], o0); o1 = fmaf(wt, rec[4 + d0 + 1], o1); }
    const size_t slot = row * NSPLIT + s;
    *reinterpret_cast<float2*>(po + slot * D + d0) = make_float2(o0, o1);
    if (lane == 0) *reinterpret_cast<float2*>(pml + 2 * slot) = make_float2(M, L);
  }
}

__global__ void __launch_bounds__(256) combine_oproj(const float* __restrict__ pml, const float* __restrict__ po, const bf16* __restrict__ wo, bf16* __restrict__ out) {
  __shared__ float o[4096];
  __shared__ float red[128][17];
  const int cgp = blockIdx.x, b = blockIdx.y, tid = threadIdx.x;
  const int kg = tid >> 1, hf = tid & 1;
  u32x4 w[32];
#pragma unroll
  for (int i = 0; i < 32; ++i) w[i] = *reinterpret_cast<const u32x4*>(wo + (size_t)(kg + 128 * i) * 512 + cgp * 16 + hf * 8);
  {
    const int h = tid >> 3, d0 = (tid & 7) * 16;
    const size_t row = (size_t)b * HQ + h;
    float M = -INFINITY;
#pragma unroll
    for (int s = 0; s < NSPLIT; ++s) M = fmaxf(M, pml[(row * NSPLIT + s) * 2]);
    float L = 0.f, a[16];
#pragma unroll
    for (int j = 0; j < 16; ++j) a[j] = 0.f;
#pragma unroll
    for (int s = 0; s < NSPLIT; ++s) {
      const float wt = __builtin_amdgcn_exp2f(pml[(row * NSPLIT + s) * 2] - M);
      L = fmaf(wt, pml[(row * NSPLIT + s) * 2 + 1], L);
#pragma unroll
      for (int j = 0; j < 16; j += 4) { const f32x4 v = *reinterpret_cast<const f32x4*>(po + (row * NSPLIT + s) * D + d0 + j);
        a[j] = fmaf(wt, v[0], a[j]); a[j + 1] = fmaf(wt, v[1], a[j + 1]); a[j + 2] = fmaf(wt, v[2], a[j + 2]); a[j + 3] = fmaf(wt, v[3], a[j + 3]); }
    }
    const float inv = 1.f / L;
#pragma unroll
    for (int j = 0; j < 16; ++j) o[h * 128 + d0 + j] = a[j] * inv;
  }
  __syncthreads();
  float acc[8];
#pragma unroll
  for (int j = 0; j < 8; ++j) acc[j] = 0.f;
#pragma unroll
  for (int i = 0; i < 32; ++i) { const float ov = o[kg + 128 * i];
#pragma unroll
    for (int j = 0; j < 4; ++j) { acc[2 * j] = fmaf(ov, bflo(w[i][j]), acc[2 * j]); acc[2 * j + 1] = fmaf(ov, bfhi(w[i][j]), acc[2 * j + 1]); } }
#pragma unroll
  for (int j = 0; j < 8; ++j) red[kg][hf * 8 + j] = acc[j];
  __syncthreads();
  if (tid < 16) { float s = 0.f; for (int k = 0; k < 128; ++k) s += red[k][tid]; out[b * 512 + cgp * 16 + tid] = __float2bfloat16(s); }
}

extern "C" void kernel_launch(void* const* d_in, const int* in_sizes, int n_in, void* d_out, int out_size, void* d_ws, size_t ws_size, hipStream_t stream) {
  const bf16* x = (const bf16*)d_in[0]; const bf16* kc = (const bf16*)d_in[1]; const bf16* vc = (const bf16*)d_in[2];
  const bf16* wq = (const bf16*)d_in[3]; const bf16* wo = (const bf16*)d_in[4];
  bf16* q = (bf16*)((char*)d_ws + WS_Q);
  float* pml = (float*)((char*)d_ws + WS_PML); float* po = (float*)((char*)d_ws + WS_PO);
  qproj<<<128, 256, 0, stream>>>(x, wq, q);
  attn_split<<<dim3(NSPLIT, HKV, B), NTHR, 0, stream>>>(q, kc, vc, pml, po);
  combine_oproj<<<dim3(32, 8), 256, 0, stream>>>(pml, po, wo, (bf16*)d_out);
}
```

```cpp
#include <hip/hip_runtime.h>
#include <hip/hip_bf16.h>
#include <cstdint>

using bf16 = __hip_bfloat16;
constexpr int B = 8, DM = 512, HQ = 32, HKV = 8, SK = 8192, D = 128, G = 4;
#define NWAVES 8
#define NSTAGE 4
#define KNT 1
#define VNT 1
constexpr int NSPLIT = 4, NW = NWAVES, NTHR = NW * 64, S = NSTAGE;
constexpr int NSUB = SK / NSPLIT / 16 / NW;
constexpr float SCALE = 0.088388347648318440f;

using bf16x8 = __attribute__((ext_vector_type(8))) __bf16;
using u32x4  = __attribute__((ext_vector_type(4))) unsigned;
using f32x4  = __attribute__((ext_vector_type(4))) float;
__device__ __forceinline__ unsigned cvtpk(float lo, float hi) {
  using f32x2_ = __attribute__((ext_vector_type(2))) float; using bf16x2_ = __attribute__((ext_vector_type(2))) __bf16;
  return __builtin_bit_cast(unsigned, __builtin_convertvector(f32x2_{lo, hi}, bf16x2_));
}
__device__ __forceinline__ float xrow16_max(float x) {
  auto s = __builtin_amdgcn_permlane16_swap(__float_as_uint(x), __float_as_uint(x), false, false);
  x = fmaxf(__uint_as_float(s[0]), __uint_as_float(s[1]));
  auto t = __builtin_amdgcn_permlane32_swap(__float_as_uint(x), __float_as_uint(x), false, false);
  return fmaxf(__uint_as_float(t[0]), __uint_as_float(t[1]));
}
__device__ __forceinline__ float xrow16_sum(float x) {
  auto s = __builtin_amdgcn_permlane16_swap(__float_as_uint(x), __float_as_uint(x), false, false);
  x = __uint_as_float(s[0]) + __uint_as_float(s[1]);
  auto t = __builtin_amdgcn_permlane32_swap(__float_as_uint(x), __float_as_uint(x), false, false);
  return __uint_as_float(t[0]) + __uint_as_float(t[1]);
}
__device__ __forceinline__ float bflo(unsigned u) { return __builtin_bit_cast(float, u << 16); }
__device__ __forceinline__ float bfhi(unsigned u) { return __builtin_bit_cast(float, u & 0xffff0000u); }
__device__ __forceinline__ f32x4 mfma16(u32x4 a, u32x4 b, f32x4 c) {
  return __builtin_amdgcn_mfma_f32_16x16x32_bf16(__builtin_bit_cast(bf16x8, a), __builtin_bit_cast(bf16x8, b), c, 0, 0, 0);
}

constexpr size_t WS_Q = 0;
constexpr size_t WS_PML = 65536;
constexpr size_t WS_PO = WS_PML + 65536;
constexpr size_t WS_TS = 1 << 20;

__global__ void __launch_bounds__(256) qproj(const bf16* __restrict__ x, const bf16* __restrict__ wq, bf16* __restrict__ q) {
  __shared__ float xs[B][DM];
  __shared__ float red[64][B * 32 + 1];
  const int tid = threadIdx.x;
  const int kg = tid >> 2, cg = tid & 3, n0 = blockIdx.x * 32 + cg * 8;
  u32x4 w[8];
#pragma unroll
  for (int i = 0; i < 8; ++i) w[i] = *reinterpret_cast<const u32x4*>(wq + (size_t)(kg + 64 * i) * 4096 + n0);
  for (int i = tid; i < B * DM; i += 256) xs[i / DM][i % DM] = __bfloat162float(x[i]);
  __syncthreads();
  float acc[B][8];
#pragma unroll
  for (int b = 0; b < B; ++b)
#pragma unroll
    for (int j = 0; j < 8; ++j) acc[b][j] = 0.f;
#pragma unroll
  for (int i = 0; i < 8; ++i) {
    float wf[8];
#pragma unroll
    for (int j = 0; j < 4; ++j) { wf[2 * j] = bflo(w[i][j]); wf[2 * j + 1] = bfhi(w[i][j]); }
#pragma unroll
    for (int b = 0; b < B; ++b) { const float xv = xs[b][kg + 64 * i];
#pragma unroll
      for (int j = 0; j < 8; ++j) acc[b][j] = fmaf(xv, wf[j], acc[b][j]); }
  }
#pragma unroll
  for (int b = 0; b < B; ++b)
#pragma unroll
    for (int j = 0; j < 8; ++j) red[kg][b * 32 + cg * 8 + j] = acc[b][j];
  __syncthreads();
  float s = 0.f;
  for (int k = 0; k < 64; ++k) s += red[k][tid];
  q[(tid >> 5) * 4096 + blockIdx.x * 32 + (tid & 31)] = __float2bfloat16(s);
}

constexpr int MRG_REC = 4 + D;
__global__ void __launch_bounds__(NTHR, NW / 4) attn_split(const bf16* __restrict__ q, const unsigned char* __restrict__ kc, const unsigned char* __restrict__ vc, float* __restrict__ pml, float* __restrict__ po
) {
  __shared__ __attribute__((aligned(16))) float mrg[NW * 4 * MRG_REC];
  const int kvh = blockIdx.x & 7, s = (blockIdx.x >> 3) & 3, b = blockIdx.x >> 5;
  const int tid = threadIdx.x, lane = tid & 63, wave = __builtin_amdgcn_readfirstlane(tid >> 6), g = lane >> 4, c = lane & 15;
  constexpr float C2 = SCALE * 1.4426950408889634f;
  const size_t base = ((size_t)(b * HKV + kvh) * SK + s * (SK / NSPLIT) + wave * 16) * 256;
  const unsigned char* kp = kc + base + (4 * (c & 3) + (c >> 2)) * 256 + g * 16;
  const unsigned char* vp = vc + base + lane * 16;
  u32x4 kb[S][4], vb[S][4];
#define LDK(p) (KNT ? __builtin_nontemporal_load((const u32x4*)(p)) : *(const u32x4*)(p))
#define LDV(p) (VNT ? __builtin_nontemporal_load((const u32x4*)(p)) : *(const u32x4*)(p))
#define ISSUE(st, u) do { const size_t o_ = (size_t)(u) * (NW * 4096); \
    _Pragma("unroll") for (int t_ = 0; t_ < 4; ++t_) kb[st][t_] = LDK(kp + o_ + t_ * 64); \
    _Pragma("unroll") for (int t_ = 0; t_ < 4; ++t_) vb[st][t_] = LDV(vp + o_ + t_ * 1024); \
    __builtin_amdgcn_sched_barrier(0); } while (0)
  u32x4 qf[4];
#pragma unroll
  for (int t = 0; t < 4; ++t) {
    const u32x4 v = *reinterpret_cast<const u32x4*>(q + ((size_t)b * HQ + kvh * G + (c & 3)) * D + (4 * t + g) * 8);
    qf[t] = c < 4 ? v : u32x4{0, 0, 0, 0};
  }
#pragma unroll
  for (int st = 0; st < S; ++st) ISSUE(st, st);
  float m = -INFINITY, l = 0.f;
  f32x4 acc[8];
#pragma unroll
  for (int i = 0; i < 8; ++i) acc[i] = f32x4{0.f, 0.f, 0.f, 0.f};
  for (int u = 0; u < NSUB; u += S) {
#pragma unroll
    for (int st = 0; st < S; st += 2) {
      f32x4 s0 = {0.f, 0.f, 0.f, 0.f}, s1 = {0.f, 0.f, 0.f, 0.f};
#pragma unroll
      for (int t = 0; t < 4; ++t) { s0 = mfma16(kb[st][t], qf[t], s0); s1 = mfma16(kb[st + 1][t], qf[t], s1); }
      float pm = fmaxf(fmaxf(fmaxf(s0[0], s0[1]), fmaxf(s0[2], s0[3])), fmaxf(fmaxf(s1[0], s1[1]), fmaxf(s1[2], s1[3])));
      pm = xrow16_max(pm);
      if (__any(pm > m)) {
        const float mn = fmaxf(m, pm);
        const float alpha = __builtin_amdgcn_exp2f((m - mn) * C2);
        m = mn; l *= alpha;
#pragma unroll
        for (int i = 0; i < 8; ++i) acc[i] *= alpha;
      }
      const float mc = -m * C2;
      float p[8];
#pragma unroll
      for (int r = 0; r < 4; ++r) { p[r] = __builtin_amdgcn_exp2f(fmaf(s0[r], C2, mc)); p[4 + r] = __builtin_amdgcn_exp2f(fmaf(s1[r], C2, mc)); }
      l += ((p[0] + p[1]) + (p[2] + p[3])) + ((p[4] + p[5]) + (p[6] + p[7]));
      const u32x4 pf = {cvtpk(p[0], p[1]), cvtpk(p[2], p[3]), cvtpk(p[4], p[5]), cvtpk(p[6], p[7])};
#pragma unroll
      for (int w = 0; w < 4; ++w) {
        u32x4 a0, a1;
        a0[0] = __builtin_amdgcn_perm(vb[st][1][w], vb[st][0][w], 0x05040100u);         a1[0] = __builtin_amdgcn_perm(vb[st][1][w], vb[st][0][w], 0x07060302u);
        a0[1] = __builtin_amdgcn_perm(vb[st][3][w], vb[st][2][w], 0x05040100u);         a1[1] = __builtin_amdgcn_perm(vb[st][3][w], vb[st][2][w], 0x07060302u);
        a0[2] = __builtin_amdgcn_perm(vb[st + 1][1][w], vb[st + 1][0][w], 0x05040100u); a1[2] = __builtin_amdgcn_perm(vb[st + 1][1][w], vb[st + 1][0][w], 0x07060302u);
        a0[3] = __builtin_amdgcn_perm(vb[st + 1][3][w], vb[st + 1][2][w], 0x05040100u); a1[3] = __builtin_amdgcn_perm(vb[st + 1][3][w], vb[st + 1][2][w], 0x07060302u);
        acc[2 * w] = mfma16(a0, pf, acc[2 * w]);
        acc[2 * w + 1] = mfma16(a1, pf, acc[2 * w + 1]);
      }
      __builtin_amdgcn_sched_barrier(0);
      if (u + S + st < NSUB) { ISSUE(st, u + S + st); ISSUE(st + 1, u + S + st + 1); }
    }
  }
#undef ISSUE
  l = xrow16_sum(l);
  if (c < 4) {
    float* rec = mrg + (wave * 4 + c) * MRG_REC;
#pragma unroll
    for (int r = 0; r < 4; ++r) {
      *reinterpret_cast<f32x4*>(rec + 4 + 8 * (4 * g + r)) = f32x4{acc[0][r], acc[1][r], acc[2][r], acc[3][r]};
      *reinterpret_cast<f32x4*>(rec + 4 + 8 * (4 * g + r) + 4) = f32x4{acc[4][r], acc[5][r], acc[6][r], acc[7][r]};
    }
    if (g == 0) { rec[0] = m * C2; rec[1] = l; }
  }
  __syncthreads();
  if (wave < 4) {
    const int h = wave, d0 = 2 * lane;
    const size_t row = (size_t)b * HQ + kvh * G + h;
    float M = -INFINITY;
#pragma unroll
    for (int w = 0; w < NW; ++w) M = fmaxf(M, mrg[(w * 4 + h) * MRG_REC]);
    float L = 0.f, o0 = 0.f, o1 = 0.f;
#pragma unroll
    for (int w = 0; w < NW; ++w) { const float* rec = mrg + (w * 4 + h) * MRG_REC; const float lw = rec[1];
      const float wt = __builtin_amdgcn_exp2f(rec[0] - M);
      L = fmaf(wt, lw, L); o0 = fmaf(wt, rec[4 + d0], o0); o1 = fmaf(wt, rec[4 + d0 + 1], o1); }
    const size_t slot = row * NSPLIT + s;
    *reinterpret_cast<float2*>(po + slot * D + d0) = make_float2(o0, o1);
    if (lane == 0) *reinterpret_cast<float2*>(pml + 2 * slot) = make_float2(M, L);
  }
}

__global__ void __launch_bounds__(256) combine_oproj(const float* __restrict__ pml, const float* __restrict__ po, const bf16* __restrict__ wo, bf16* __restrict__ out) {
  __shared__ float o[4096];
  __shared__ float red[128][17];
  const int j = blockIdx.x & 31, cgp = (j & 7) * 4 + (j >> 3), b = blockIdx.x >> 5, tid = threadIdx.x;
  const int kg = tid >> 1, hf = tid & 1;
  u32x4 w[32];
#pragma unroll
  for (int i = 0; i < 32; ++i) w[i] = *reinterpret_cast<const u32x4*>(wo + (size_t)(kg + 128 * i) * 512 + cgp * 16 + hf * 8);
  {
    const int h = tid >> 3, d0 = (tid & 7) * 16;
    const size_t row = (size_t)b * HQ + h;
    float M = -INFINITY;
#pragma unroll
    for (int s = 0; s < NSPLIT; ++s) M = fmaxf(M, pml[(row * NSPLIT + s) * 2]);
    float L = 0.f, a[16];
#pragma unroll
    for (int j = 0; j < 16; ++j) a[j] = 0.f;
#pragma unroll
    for (int s = 0; s < NSPLIT; ++s) {
      const float wt = __builtin_amdgcn_exp2f(pml[(row * NSPLIT + s) * 2] - M);
      L = fmaf(wt, pml[(row * NSPLIT + s) * 2 + 1], L);
#pragma unroll
      for (int j = 0; j < 16; j += 4) { const f32x4 v = *reinterpret_cast<const f32x4*>(po + (row * NSPLIT + s) * D + d0 + j);
        a[j] = fmaf(wt, v[0], a[j]); a[j + 1] = fmaf(wt, v[1], a[j + 1]); a[j + 2] = fmaf(wt, v[2], a[j + 2]); a[j + 3] = fmaf(wt, v[3], a[j + 3]); }
    }
    const float inv = 1.f / L;
#pragma unroll
    for (int j = 0; j < 16; ++j) o[h * 128 + d0 + j] = a[j] * inv;
  }
  __syncthreads();
  float acc[8];
#pragma unroll
  for (int j = 0; j < 8; ++j) acc[j] = 0.f;
#pragma unroll
  for (int i = 0; i < 32; ++i) { const float ov = o[kg + 128 * i];
#pragma unroll
    for (int j = 0; j < 4; ++j) { acc[2 * j] = fmaf(ov, bflo(w[i][j]), acc[2 * j]); acc[2 * j + 1] = fmaf(ov, bfhi(w[i][j]), acc[2 * j + 1]); } }
#pragma unroll
  for (int j = 0; j < 8; ++j) red[kg][hf * 8 + j] = acc[j];
  __syncthreads();
  if (tid < 16) { float s = 0.f; for (int k = 0; k < 128; ++k) s += red[k][tid]; out[b * 512 + cgp * 16 + tid] = __float2bfloat16(s); }
}

extern "C" void kernel_launch(void* const* d_in, const int* in_sizes, int n_in, void* d_out, int out_size, void* d_ws, size_t ws_size, hipStream_t stream) {
  const bf16* x = (const bf16*)d_in[0]; const unsigned char* kc = (const unsigned char*)d_in[1]; const unsigned char* vc = (const unsigned char*)d_in[2];
  const bf16* wq = (const bf16*)d_in[3]; const bf16* wo = (const bf16*)d_in[4];
  bf16* q = (bf16*)((char*)d_ws + WS_Q);
  float* pml = (float*)((char*)d_ws + WS_PML); float* po = (float*)((char*)d_ws + WS_PO);
  qproj<<<128, 256, 0, stream>>>(x, wq, q);
  attn_split<<<256, NTHR, 0, stream>>>(q, kc, vc, pml, po
  );
  combine_oproj<<<256, 256, 0, stream>>>(pml, po, wo, (bf16*)d_out);
}
```

```cpp
#define DIAG 1
#define LATEW 12
#define QSLEEP 32
#include <hip/hip_runtime.h>
#include <hip/hip_bf16.h>
#include <cstdint>
#include <type_traits>

#define PSLEEP 0
#define RGATE 0
#define GATE 7
#define HIT 1
#define HPRIO 0
constexpr int SK = 8192, NW = 8, NTHR = NW * 64;
constexpr float C2 = 0.088388347648318440f * 1.4426950408889634f;
constexpr unsigned SPIN_CAP = 1u << 16;

using bf16x8 = __attribute__((ext_vector_type(8))) __bf16;
using bf16x2 = __attribute__((ext_vector_type(2))) __bf16;
using u32x4  = __attribute__((ext_vector_type(4))) unsigned;
using u32x2  = __attribute__((ext_vector_type(2))) unsigned;
using f32x4  = __attribute__((ext_vector_type(4))) float;
using f32x2  = __attribute__((ext_vector_type(2))) float;
using rsrc_t = __amdgpu_buffer_rsrc_t;
#define LAS __attribute__((address_space(3)))
#define GAS __attribute__((address_space(1)))

__device__ __forceinline__ unsigned cvtpk(float lo, float hi) { return __builtin_bit_cast(unsigned, __builtin_convertvector(f32x2{lo, hi}, bf16x2)); }
__device__ __forceinline__ float bflo(unsigned u) { return __builtin_bit_cast(float, u << 16); }
__device__ __forceinline__ float bfhi(unsigned u) { return __builtin_bit_cast(float, u & 0xffff0000u); }
__device__ __forceinline__ f32x4 mfma16(u32x4 a, u32x4 b, f32x4 c) {
  return __builtin_amdgcn_mfma_f32_16x16x32_bf16(__builtin_bit_cast(bf16x8, a), __builtin_bit_cast(bf16x8, b), c, 0, 0, 0);
}
template <int CTRL> __device__ __forceinline__ float dpp(float x) {
  return __builtin_bit_cast(float, __builtin_amdgcn_mov_dpp(__builtin_bit_cast(int, x), CTRL, 0xf, 0xf, true));
}
template <int CTRL> __device__ __forceinline__ unsigned dppu(unsigned x) { return (unsigned)__builtin_amdgcn_mov_dpp((int)x, CTRL, 0xf, 0xf, true); }
__device__ __forceinline__ float xrow_max(float x) {
  auto s = __builtin_amdgcn_permlane16_swap(__float_as_uint(x), __float_as_uint(x), false, false);
  x = fmaxf(__uint_as_float(s[0]), __uint_as_float(s[1]));
  auto t = __builtin_amdgcn_permlane32_swap(__float_as_uint(x), __float_as_uint(x), false, false);
  return fmaxf(__uint_as_float(t[0]), __uint_as_float(t[1]));
}
__device__ __forceinline__ float xrow_sum(float x) {
  auto s = __builtin_amdgcn_permlane16_swap(__float_as_uint(x), __float_as_uint(x), false, false);
  x = __uint_as_float(s[0]) + __uint_as_float(s[1]);
  auto t = __builtin_amdgcn_permlane32_swap(__float_as_uint(x), __float_as_uint(x), false, false);
  return __uint_as_float(t[0]) + __uint_as_float(t[1]);
}
__device__ __forceinline__ rsrc_t mkrsrc(const void* p, unsigned bytes) { return __builtin_amdgcn_make_buffer_rsrc(const_cast<void*>(p), 0, bytes, 0x00020000); }
#define PLO 0x05040100u
#define PHI 0x07060302u
#define LDSWAIT() asm volatile("s_waitcnt lgkmcnt(0)" ::: "memory")
#define AUX_NT 2
#define AUX_SC1 16

constexpr unsigned WS_EPOCH = 0;
#define PAD 256
constexpr unsigned QS = 16384 + 2 * PAD, OS = 8192 + PAD, LS = 4096 + PAD, YS = 8192 + PAD, YBS = 32 * YS + 4 * PAD;
constexpr unsigned WS_QG = 4096;
constexpr unsigned WS_OG = WS_QG + 64 * QS;
constexpr unsigned WS_LG = WS_OG + 64 * OS;
constexpr unsigned WS_YG = WS_LG + 32 * LS;
constexpr unsigned WS_END = WS_YG + 8 * YBS;
constexpr unsigned WS_TS = 8 << 20;
constexpr int MREC = (4 + 128) * 4;
constexpr int L_M = 0;
constexpr int L_QT = L_M + NW * 4 * MREC;
constexpr int L_Q = L_QT + 4096;
constexpr int L_F = L_Q + 1024;
constexpr int L_T = L_F + 32;
constexpr int L_R = L_T + 8 * 4 * 144;
constexpr int L_W = L_R + 8 * 64;
constexpr int L_END = L_W + 16384;

__global__ void __launch_bounds__(NTHR, 2) fused_gqa(const unsigned char* __restrict__ x, const unsigned char* __restrict__ kc, const unsigned char* __restrict__ vc,
                                                      const unsigned char* __restrict__ wq, const unsigned char* __restrict__ wo, unsigned char* __restrict__ out, unsigned char* ws) {
  __shared__ __attribute__((aligned(16))) unsigned char lds_raw[L_END];
  LAS unsigned char* lds = (LAS unsigned char*)lds_raw;
  volatile LAS unsigned* flg = (volatile LAS unsigned*)(lds + L_F);
#define TS(i)
  TS(0);
  const unsigned long long dg0 = __builtin_amdgcn_s_memrealtime(); unsigned long long dgm = 0;
  asm volatile("" : "+s"(x), "+s"(kc), "+s"(vc), "+s"(wq), "+s"(wo), "+s"(out), "+s"(ws));
  const int blk = blockIdx.x, j = blk & 31, b = blk >> 5, kvh = j & 7, s = j >> 3;
  const int tid = threadIdx.x, lane = tid & 63, wave = __builtin_amdgcn_readfirstlane(tid >> 6), g = lane >> 4, c = lane & 15;
  if (tid < 8) flg[tid] = 0u;
  const size_t kvoff = ((size_t)(b * 8 + kvh) * SK + s * (SK / 4)) * 256;
  const rsrc_t rk = mkrsrc(kc + kvoff, 2048 * 256), rv = mkrsrc(vc + kvoff, 2048 * 256);
  const rsrc_t rws = mkrsrc(ws, WS_END);
  unsigned kvo = (4 * (c & 3) + (c >> 2)) * 256 + g * 16, vvo = lane * 16;
  u32x4 kb[4][4], vb[4][4];
#define BURST 64
#define BRK "s_nop 0"
#define BREAK(n) do { if ((n) % BURST == 0) { __builtin_amdgcn_sched_barrier(0); asm volatile(BRK); __builtin_amdgcn_sched_barrier(0); } } while (0)
#define ISSUE_KV(st, so) do { \
    _Pragma("unroll") for (int t_ = 0; t_ < 4; ++t_) { kb[st][t_] = __builtin_amdgcn_raw_buffer_load_b128(rk, kvo + t_ * 64, (so), AUX_NT); BREAK((st) * 8 + t_ + 1); } \
    _Pragma("unroll") for (int t_ = 0; t_ < 4; ++t_) { vb[st][t_] = __builtin_amdgcn_raw_buffer_load_b128(rv, vvo + t_ * 1024, (so), AUX_NT); BREAK((st) * 8 + t_ + 5); } \
    __builtin_amdgcn_sched_barrier(0); } while (0)
  bool last = false;
  const int kh = b & 1, cq = b >> 1, H = kvh * 4 + s;
  unsigned tag;

  if (wave == 0) {
    __builtin_amdgcn_s_setprio(3);
    const unsigned ep = __builtin_amdgcn_raw_buffer_load_b32(rws, 0, WS_EPOCH, AUX_SC1);
    const rsrc_t rx = mkrsrc(x, 8192), rq = mkrsrc(wq + (size_t)s * (128 * 8192) + (kvh * 8 + b) * 128, 128 * 8192);
    u32x4 xf[2], wqb[16];
    const unsigned xvo = (c & 7) * 1024 + (c >> 3) * 128 + g * 16;
    xf[0] = __builtin_amdgcn_raw_buffer_load_b128(rx, xvo, s * 256, 0);
    xf[1] = __builtin_amdgcn_raw_buffer_load_b128(rx, xvo + 64, s * 256, 0);
    const unsigned qvo = ((c >> 3) * 64 + g * 8) * 8192 + (c & 7) * 16;
#pragma unroll
    for (int i = 0; i < 16; ++i) wqb[i] = __builtin_amdgcn_raw_buffer_load_b128(rq, qvo, ((i >> 3) * 32 + (i & 7)) * 8192, AUX_NT);
    __builtin_amdgcn_sched_barrier(0);
    LDSWAIT(); __builtin_amdgcn_s_barrier(); asm volatile("" ::: "memory");
    __builtin_amdgcn_sched_barrier(0);
    tag = __builtin_amdgcn_readfirstlane(ep) + 0x9E3779B9u;
    f32x4 qa[8];
#pragma unroll
    for (int i = 0; i < 8; ++i) qa[i] = f32x4{0.f, 0.f, 0.f, 0.f};
#pragma unroll
    for (int t = 0; t < 2; ++t)
#pragma unroll
      for (int w = 0; w < 4; ++w) {
        u32x4 a0, a1;
#pragma unroll
        for (int a = 0; a < 4; ++a) { a0[a] = __builtin_amdgcn_perm(wqb[8 * t + 2 * a + 1][w], wqb[8 * t + 2 * a][w], PLO); a1[a] = __builtin_amdgcn_perm(wqb[8 * t + 2 * a + 1][w], wqb[8 * t + 2 * a][w], PHI); }
        qa[2 * w] = mfma16(a0, xf[t], qa[2 * w]); qa[2 * w + 1] = mfma16(a1, xf[t], qa[2 * w + 1]);
      }
    if ((c >> 3) == (g >> 1)) {
#pragma unroll
      for (int r = 0; r < 4; ++r) {
        *(LAS f32x4*)(lds + L_QT + c * 256 + (4 * (g & 1) + r) * 32) = f32x4{qa[0][r], qa[1][r], qa[2][r], qa[3][r]};
        *(LAS f32x4*)(lds + L_QT + c * 256 + (4 * (g & 1) + r) * 32 + 16) = f32x4{qa[4][r], qa[5][r], qa[6][r], qa[7][r]};
      }
    }
    LDSWAIT();
    {
      const f32x4 u0 = *(LAS f32x4*)(lds + L_QT + lane * 32) + *(LAS f32x4*)(lds + L_QT + 2048 + lane * 32);
      const f32x4 u1 = *(LAS f32x4*)(lds + L_QT + lane * 32 + 16) + *(LAS f32x4*)(lds + L_QT + 2048 + lane * 32 + 16);
      const unsigned vo = (lane >> 3) * QS + (lane & 7) * 16, so = WS_QG + kvh * 8 * QS + s * 4096 + b * 512;
      u32x4 d0 = {__float_as_uint(u0[0]), tag, __float_as_uint(u0[1]), tag}, d1 = {__float_as_uint(u0[2]), tag, __float_as_uint(u0[3]), tag};
      u32x4 d2 = {__float_as_uint(u1[0]), tag, __float_as_uint(u1[1]), tag}, d3 = {__float_as_uint(u1[2]), tag, __float_as_uint(u1[3]), tag};
      asm volatile("" : "+v"(d0), "+v"(d1), "+v"(d2), "+v"(d3));
      __builtin_amdgcn_sched_barrier(0);
      __builtin_amdgcn_raw_buffer_store_b128(d0, rws, vo, so, AUX_SC1);
      __builtin_amdgcn_raw_buffer_store_b128(d1, rws, vo + 128, so, AUX_SC1);
      __builtin_amdgcn_raw_buffer_store_b128(d2, rws, vo + 256, so, AUX_SC1);
      __builtin_amdgcn_raw_buffer_store_b128(d3, rws, vo + 384, so, AUX_SC1);
      __builtin_amdgcn_sched_barrier(0);
    }
    TS(1);
    __builtin_amdgcn_s_sleep(QSLEEP);
    u32x4 qg[16];
#pragma unroll 1
    for (unsigned spin = 0; spin < SPIN_CAP; ++spin) {
      asm volatile("" ::: "memory");
      bool ok = true;
#pragma unroll
      for (int i = 0; i < 16; ++i) qg[i] = __builtin_amdgcn_raw_buffer_load_b128(rws, lane * 16 + (i & 3) * 1024, WS_QG + (kvh * 8 + b) * QS + (i >> 2) * 4096, AUX_SC1);
#pragma unroll
      for (int i = 0; i < 16; ++i) ok = ok & (qg[i][1] == tag) & (qg[i][3] == tag);
      if (__all(ok)) break;
    }
#pragma unroll
    for (int h = 0; h < 4; ++h) {
      const float q0 = (__uint_as_float(qg[h][0]) + __uint_as_float(qg[4 + h][0])) + (__uint_as_float(qg[8 + h][0]) + __uint_as_float(qg[12 + h][0]));
      const float q1 = (__uint_as_float(qg[h][2]) + __uint_as_float(qg[4 + h][2])) + (__uint_as_float(qg[8 + h][2]) + __uint_as_float(qg[12 + h][2]));
      *(LAS unsigned*)(lds + L_Q + h * 256 + (lane >> 5) * 128 + (lane & 7) * 16 + ((lane >> 3) & 3) * 4) = cvtpk(q0, q1);
    }
    flg[3] = tag;
    LDSWAIT();
    flg[0] = 1u;
    if (DIAG == 3) dgm = 4 * (__builtin_amdgcn_s_memrealtime() - dg0);
    TS(2);
    __builtin_amdgcn_s_setprio(0);
    {
#pragma unroll 1
      for (unsigned spin = 0; spin < (SPIN_CAP << 4); ++spin) { if (flg[2] >= LATEW) break; __builtin_amdgcn_s_sleep(4); }
      asm volatile("" ::: "memory");
      const rsrc_t rw = mkrsrc(wo + (size_t)(H * 128 + kh * 64) * 1024 + cq * 256, 65536);
      u32x4 wr[16];
#pragma unroll
      for (int i = 0; i < 16; ++i) wr[i] = __builtin_amdgcn_raw_buffer_load_b128(rw, g * 8192 + c * 16 + (i & 3) * 1024, ((i >> 3) * 32 + (i & 4)) * 1024, AUX_NT);
#pragma unroll
      for (int t = 0; t < 2; ++t)
#pragma unroll
        for (int w = 0; w < 4; ++w) {
          u32x4 f0, f1;
#pragma unroll
          for (int a = 0; a < 4; ++a) { f0[a] = __builtin_amdgcn_perm(wr[8 * t + 2 * a + 1][w], wr[8 * t + 2 * a][w], PLO); f1[a] = __builtin_amdgcn_perm(wr[8 * t + 2 * a + 1][w], wr[8 * t + 2 * a][w], PHI); }
          *(LAS u32x4*)(lds + L_W + (8 * t + 2 * w) * 1024 + lane * 16) = f0;
          *(LAS u32x4*)(lds + L_W + (8 * t + 2 * w + 1) * 1024 + lane * 16) = f1;
        }
      LDSWAIT();
      flg[4] = 1u;
    }
  }
  else {
  asm volatile("" : "+v"(kvo), "+v"(vvo));
  const int w0 = (wave - 1) * 4096, d0 = 7 * 4096;
  __builtin_amdgcn_sched_barrier(0);
  __builtin_amdgcn_s_barrier(); asm volatile("" ::: "memory");
  __builtin_amdgcn_sched_barrier(0);
  ISSUE_KV(0, w0);
  ISSUE_KV(1, w0 + d0); ISSUE_KV(2, w0 + 2 * d0); ISSUE_KV(3, w0 + 3 * d0);
#pragma unroll 1
  for (unsigned spin = 0; spin < (SPIN_CAP << 6); ++spin) { if (flg[0] != 0u) break; __builtin_amdgcn_s_sleep(1); }
  asm volatile("" ::: "memory");
  TS(2);
  tag = __builtin_amdgcn_readfirstlane(flg[3]);
  u32x4 qf[4];
#pragma unroll
  for (int t = 0; t < 4; ++t) qf[t] = *(LAS u32x4*)(lds + L_Q + (c & 3) * 256 + (4 * t + g) * 16);

  float m = -INFINITY, l = 0.f;
  f32x4 acc[8];
#pragma unroll
  for (int i = 0; i < 8; ++i) acc[i] = f32x4{0.f, 0.f, 0.f, 0.f};
  const int nit = wave < 4 ? 1 : wave == 4 ? 5 : 8;
  auto tile = [&](auto stc) __attribute__((always_inline)) {
      constexpr int st = decltype(stc)::value;
      f32x4 s0 = {0.f, 0.f, 0.f, 0.f}, s1 = {0.f, 0.f, 0.f, 0.f};
#pragma unroll
      for (int t = 0; t < 4; ++t) { s0 = mfma16(kb[st][t], qf[t], s0); s1 = mfma16(kb[st + 1][t], qf[t], s1); }
      float pm = fmaxf(fmaxf(fmaxf(s0[0], s0[1]), fmaxf(s0[2], s0[3])), fmaxf(fmaxf(s1[0], s1[1]), fmaxf(s1[2], s1[3])));
      pm = xrow_max(pm);
      if (__any(pm > m)) {
        const float mn = fmaxf(m, pm);
        const float alpha = __builtin_amdgcn_exp2f((m - mn) * C2);
        m = mn; l *= alpha;
#pragma unroll
        for (int i = 0; i < 8; ++i) acc[i] *= alpha;
      }
      const float mc = -m * C2;
      float p[8];
#pragma unroll
      for (int r = 0; r < 4; ++r) { p[r] = __builtin_amdgcn_exp2f(fmaf(s0[r], C2, mc)); p[4 + r] = __builtin_amdgcn_exp2f(fmaf(s1[r], C2, mc)); }
      l += ((p[0] + p[1]) + (p[2] + p[3])) + ((p[4] + p[5]) + (p[6] + p[7]));
      const u32x4 pf = {cvtpk(p[0], p[1]), cvtpk(p[2], p[3]), cvtpk(p[4], p[5]), cvtpk(p[6], p[7])};
#pragma unroll
      for (int w = 0; w < 4; ++w) {
        u32x4 a0, a1;
        a0[0] = __builtin_amdgcn_perm(vb[st][1][w], vb[st][0][w], PLO);         a1[0] = __builtin_amdgcn_perm(vb[st][1][w], vb[st][0][w], PHI);
        a0[1] = __builtin_amdgcn_perm(vb[st][3][w], vb[st][2][w], PLO);         a1[1] = __builtin_amdgcn_perm(vb[st][3][w], vb[st][2][w], PHI);
        a0[2] = __builtin_amdgcn_perm(vb[st + 1][1][w], vb[st + 1][0][w], PLO); a1[2] = __builtin_amdgcn_perm(vb[st + 1][1][w], vb[st + 1][0][w], PHI);
        a0[3] = __builtin_amdgcn_perm(vb[st + 1][3][w], vb[st + 1][2][w], PLO); a1[3] = __builtin_amdgcn_perm(vb[st + 1][3][w], vb[st + 1][2][w], PHI);
        acc[2 * w] = mfma16(a0, pf, acc[2 * w]);
        acc[2 * w + 1] = mfma16(a1, pf, acc[2 * w + 1]);
      }
      __builtin_amdgcn_sched_barrier(0);
  };
#pragma unroll 1
  for (int it = 0; it < nit - 1; ++it) {
    const int so0 = (it < 4 ? 28 + it * 16 + wave - 4 : 92 + (it - 4) * 12 + wave - 5) * 4096, dso = it < 4 ? 16384 : 12288;
    tile(std::integral_constant<int, 0>{});
    ISSUE_KV(0, so0); ISSUE_KV(1, so0 + dso);
    tile(std::integral_constant<int, 2>{});
    ISSUE_KV(2, so0 + 2 * dso); ISSUE_KV(3, so0 + 3 * dso);
  }
  if (lane == 0) __hip_atomic_fetch_add((LAS unsigned*)(lds + L_F + 8), 1u, __ATOMIC_RELAXED, __HIP_MEMORY_SCOPE_WORKGROUP);
  tile(std::integral_constant<int, 0>{});
  if (lane == 0) __hip_atomic_fetch_add((LAS unsigned*)(lds + L_F + 8), 1u, __ATOMIC_RELAXED, __HIP_MEMORY_SCOPE_WORKGROUP);
  tile(std::integral_constant<int, 2>{});
  l = xrow_sum(l);
  if (c < 4) {
    LAS unsigned char* rec = lds + L_M + (wave * 4 + c) * MREC;
#pragma unroll
    for (int r = 0; r < 4; ++r) {
      *(LAS f32x4*)(rec + 16 + 32 * (4 * g + r)) = f32x4{acc[0][r], acc[1][r], acc[2][r], acc[3][r]};
      *(LAS f32x4*)(rec + 16 + 32 * (4 * g + r) + 16) = f32x4{acc[4][r], acc[5][r], acc[6][r], acc[7][r]};
    }
    if (g == 0) *(LAS f32x2*)rec = f32x2{m * C2, l};
  }
  LDSWAIT();
  unsigned arr = 0;
  if (lane == 0) arr = __hip_atomic_fetch_add((LAS unsigned*)(lds + L_F + 4), 1u, __ATOMIC_RELAXED, __HIP_MEMORY_SCOPE_WORKGROUP);
  last = __builtin_amdgcn_readfirstlane(arr) == NW - 2;
  asm volatile("" ::: "memory");
  TS(3);
  }
  if (last) {
    __builtin_amdgcn_s_setprio(3);
    f32x2 ml[NW]; f32x4 a0[NW], a1[NW];
#pragma unroll
    for (int w = 1; w < NW; ++w) { LAS unsigned char* rec = lds + L_M + (w * 4 + g) * MREC; ml[w] = *(LAS f32x2*)rec; a0[w] = *(LAS f32x4*)(rec + 16 + c * 32); a1[w] = *(LAS f32x4*)(rec + 32 + c * 32); }
    float M = ml[1][0];
#pragma unroll
    for (int w = 2; w < NW; ++w) M = fmaxf(M, ml[w][0]);
    float L = 0.f; f32x4 o0 = {0.f, 0.f, 0.f, 0.f}, o1 = {0.f, 0.f, 0.f, 0.f};
#pragma unroll
    for (int w = 1; w < NW; ++w) { const float wt = __builtin_amdgcn_exp2f(ml[w][0] - M); L = fmaf(wt, ml[w][1], L); o0 += wt * a0[w]; o1 += wt * a1[w]; }
    const float inv = __builtin_amdgcn_rcpf(L);
    o0 *= inv; o1 *= inv;
    const unsigned so = WS_OG + kvh * 8 * OS + (b * 4 + s) * 256, vo = (lane >> 3) * OS + (lane & 7) * 16;
    u32x4 d0 = {cvtpk(o0[0], o0[1]), tag, cvtpk(o0[2], o0[3]), tag}, d1 = {cvtpk(o1[0], o1[1]), tag, cvtpk(o1[2], o1[3]), tag};
    const unsigned lse = __float_as_uint(M + __builtin_amdgcn_logf(L));
    u32x4 d2 = {lse, tag, lse, tag};
    __builtin_amdgcn_raw_buffer_store_b128(d0, rws, vo, so, AUX_SC1);
    __builtin_amdgcn_raw_buffer_store_b128(d1, rws, vo + 128, so, AUX_SC1);
    __builtin_amdgcn_raw_buffer_store_b128(d2, rws, c < 8 ? g * LS + c * 16 : 0x40000000, WS_LG + kvh * 4 * LS + (b * 4 + s) * 128, AUX_SC1);
    TS(4);
    if (DIAG == 4) dgm = 2 * (__builtin_amdgcn_s_memrealtime() - dg0);
  }
  {
#pragma unroll 1
    for (unsigned spin = 0; spin < (SPIN_CAP << 4); ++spin) { if (flg[4] != 0u) break; __builtin_amdgcn_s_sleep(2); }
    asm volatile("" ::: "memory");
    u32x4 wf[16];
#pragma unroll
    for (int i = 0; i < 16; ++i) wf[i] = *(LAS u32x4*)(lds + L_W + i * 1024 + lane * 16);
#pragma unroll 1
    for (unsigned spin = 0; spin < (SPIN_CAP << 4); ++spin) { if (flg[1] >= (unsigned)GATE) break; __builtin_amdgcn_s_sleep(2); }
    u32x4 og, lg;
    LAS unsigned char* tb = lds + L_T + wave * (4 * 144);
#pragma unroll 1
    for (unsigned spin = 0; spin < SPIN_CAP; ++spin) {
      asm volatile("" ::: "memory");
      og = __builtin_amdgcn_raw_buffer_load_b128(rws, lane * 16, WS_OG + (H * 2 + kh) * OS + wave * 1024, AUX_SC1);
      lg = __builtin_amdgcn_raw_buffer_load_b128(rws, (c & 3) * 128, WS_LG + H * LS + wave * 512, AUX_SC1);
      if (__all((og[1] == tag) & (og[3] == tag) & (lg[1] == tag) & (lg[3] == tag))) break;
    }
    TS(5);
    *(LAS u32x2*)(tb + g * 144 + (c & 7) * 16 + (c >> 3) * 8) = u32x2{og[0], og[2]};
    LDSWAIT();
    const u32x4 a0 = *(LAS u32x4*)(tb + (c & 3) * 144 + g * 16), a1 = *(LAS u32x4*)(tb + (c & 3) * 144 + 64 + g * 16);
    const float lse = __uint_as_float(lg[0]);
    float mx = fmaxf(lse, dpp<0xB1>(lse)); mx = fmaxf(mx, dpp<0x4E>(mx));
    const float e = __builtin_amdgcn_exp2f(lse - mx);
    float sm = e + dpp<0xB1>(e); sm += dpp<0x4E>(sm);
    const float wt = e * __builtin_amdgcn_rcpf(sm);
    const float w0 = dpp<0x00>(wt), w1 = dpp<0x55>(wt), w2 = dpp<0xAA>(wt), w3 = dpp<0xFF>(wt);
    float y[8];
#pragma unroll
    for (int i = 0; i < 8; ++i) {
      f32x4 d = mfma16(a0, wf[i], f32x4{0.f, 0.f, 0.f, 0.f});
      d = mfma16(a1, wf[8 + i], d);
      y[i] = fmaf(w3, d[3], fmaf(w2, d[2], fmaf(w1, d[1], w0 * d[0])));
    }
    const float y0 = (g & 2) ? ((g & 1) ? y[6] : y[4]) : ((g & 1) ? y[2] : y[0]);
    const float y1 = (g & 2) ? ((g & 1) ? y[7] : y[5]) : ((g & 1) ? y[3] : y[1]);
    __builtin_amdgcn_raw_buffer_store_b128(u32x4{__float_as_uint(y0), tag, __float_as_uint(y1), tag}, rws, (lane >> 3) * YS + (lane & 7) * 16, WS_YG + wave * YBS + cq * 8 * YS + (H * 2 + kh) * 128, AUX_SC1);
    TS(6);
  }
  {
    u32x4 yg;
#pragma unroll 1
    for (unsigned spin = 0; spin < SPIN_CAP; ++spin) {
      asm volatile("" ::: "memory");
      yg = __builtin_amdgcn_raw_buffer_load_b128(rws, lane * 16, WS_YG + b * YBS + j * YS + wave * 1024, AUX_SC1);
      if (__all((yg[1] == tag) & (yg[3] == tag))) break;
    }
    float r0 = __uint_as_float(yg[0]), r1 = __uint_as_float(yg[2]);
    r0 += dpp<0x128>(r0); r1 += dpp<0x128>(r1);
    r0 = xrow_sum(r0); r1 = xrow_sum(r1);
    if (lane < 8) *(LAS f32x2*)(lds + L_R + wave * 64 + lane * 8) = f32x2{r0, r1};
    LDSWAIT();
    unsigned arr = 0;
    if (lane == 0) arr = __hip_atomic_fetch_add((LAS unsigned*)(lds + L_F + 20), 1u, __ATOMIC_RELAXED, __HIP_MEMORY_SCOPE_WORKGROUP);
    if (__builtin_amdgcn_readfirstlane(arr) == NW - 1) {
      asm volatile("" ::: "memory");
      const f32x2 p = *(LAS f32x2*)(lds + L_R + lane * 8);
      r0 = p[0]; r1 = p[1];
      r0 += dpp<0x128>(r0); r1 += dpp<0x128>(r1);
      r0 = xrow_sum(r0); r1 = xrow_sum(r1);
      if (lane < 8) *(unsigned*)(out + b * 1024 + (j >> 3) * 256 + ((j & 1) * 8 + lane) * 16 + ((j >> 1) & 3) * 4) = cvtpk(r0, r1);
      if (lane == 0 && blk == 0) __hip_atomic_store((GAS unsigned*)(ws + WS_EPOCH), tag, __ATOMIC_RELAXED, __HIP_MEMORY_SCOPE_AGENT);
      TS(7);
      if (DIAG == 5) dgm = 2 * (__builtin_amdgcn_s_memrealtime() - dg0);
    }
  }
  if (DIAG == 2) { const int a = __builtin_ctzll((unsigned long long)kc), bb = __builtin_ctzll((unsigned long long)vc); dgm = 400 * (a < bb ? a : bb); }
  if (DIAG == 6) { const int a = __builtin_ctzll((unsigned long long)ws | (unsigned long long)wq | (unsigned long long)wo); dgm = 400 * a; }
  while (__builtin_amdgcn_s_memrealtime() < dg0 + 10000 + dgm) __builtin_amdgcn_s_sleep(4);
}

extern "C" void kernel_launch(void* const* d_in, const int* in_sizes, int n_in, void* d_out, int out_size, void* d_ws, size_t ws_size, hipStream_t stream) {
  fused_gqa<<<256, NTHR, 0, stream>>>((const unsigned char*)d_in[0], (const unsigned char*)d_in[1], (const unsigned char*)d_in[2], (const unsigned char*)d_in[3], (const unsigned char*)d_in[4],
                                      (unsigned char*)d_out, (unsigned char*)d_ws);
}
```

```cpp
#include <hip/hip_runtime.h>
#include <hip/hip_bf16.h>
#include <cstdint>
#include <type_traits>

#define LATEW 8
#define WQSZ (32 * 8192)
#define WREORD 0
#define PREMERGE 1
#define MASKED 2
#define EARLYKV 1
#define WAUX 0
#define PSLEEP 6
#define PSLEEP2 3
#define PREWARM 1
constexpr int SK = 8192, NW = 8, NTHR = NW * 64;
constexpr float C2 = 0.088388347648318440f * 1.4426950408889634f;
constexpr unsigned SPIN_CAP = 1u << 16;
constexpr unsigned OOB = 0x40000000u;

using bf16x8 = __attribute__((ext_vector_type(8))) __bf16;
using bf16x2 = __attribute__((ext_vector_type(2))) __bf16;
using u32x4  = __attribute__((ext_vector_type(4))) unsigned;
using u32x2  = __attribute__((ext_vector_type(2))) unsigned;
using f32x4  = __attribute__((ext_vector_type(4))) float;
using f32x2  = __attribute__((ext_vector_type(2))) float;
using rsrc_t = __amdgpu_buffer_rsrc_t;
#define LAS __attribute__((address_space(3)))
#define GAS __attribute__((address_space(1)))

__device__ __forceinline__ unsigned cvtpk(float lo, float hi) { return __builtin_bit_cast(unsigned, __builtin_convertvector(f32x2{lo, hi}, bf16x2)); }
__device__ __forceinline__ f32x4 mfma16(u32x4 a, u32x4 b, f32x4 c) {
  return __builtin_amdgcn_mfma_f32_16x16x32_bf16(__builtin_bit_cast(bf16x8, a), __builtin_bit_cast(bf16x8, b), c, 0, 0, 0);
}
template <int CTRL> __device__ __forceinline__ float dpp(float x) {
  return __builtin_bit_cast(float, __builtin_amdgcn_mov_dpp(__builtin_bit_cast(int, x), CTRL, 0xf, 0xf, true));
}
__device__ __forceinline__ float xrow_max(float x) {
  auto s = __builtin_amdgcn_permlane16_swap(__float_as_uint(x), __float_as_uint(x), false, false);
  x = fmaxf(__uint_as_float(s[0]), __uint_as_float(s[1]));
  auto t = __builtin_amdgcn_permlane32_swap(__float_as_uint(x), __float_as_uint(x), false, false);
  return fmaxf(__uint_as_float(t[0]), __uint_as_float(t[1]));
}
__device__ __forceinline__ float xrow_sum(float x) {
  auto s = __builtin_amdgcn_permlane16_swap(__float_as_uint(x), __float_as_uint(x), false, false);
  x = __uint_as_float(s[0]) + __uint_as_float(s[1]);
  auto t = __builtin_amdgcn_permlane32_swap(__float_as_uint(x), __float_as_uint(x), false, false);
  return __uint_as_float(t[0]) + __uint_as_float(t[1]);
}
__device__ __forceinline__ rsrc_t mkrsrc(const void* p, unsigned bytes) { return __builtin_amdgcn_make_buffer_rsrc(const_cast<void*>(p), 0, bytes, 0x00020000); }
#define PLO 0x05040100u
#define PHI 0x07060302u
#define CFENCE() asm volatile("" ::: "memory")
#define SB() __builtin_amdgcn_sched_barrier(0)
#define AUX_NT 2
#define AUX_SC1 16

constexpr unsigned QS = 16384 + 512, OS = 8192 + 256, LS = 4096 + 256;
constexpr unsigned WS_EPOCH = 0;
constexpr unsigned WS_QG = 4096;
constexpr unsigned WS_OG = WS_QG + 64 * QS;
constexpr unsigned WS_LG = WS_OG + 64 * OS;
constexpr unsigned WS_END = WS_LG + 32 * LS;
constexpr unsigned WS_TS = 8 << 20;
constexpr int MREC = (4 + 128) * 4;
constexpr int L_M = 0;
constexpr int L_QT = L_M + NW * 4 * MREC;
constexpr int L_Q = L_QT + 8192;
constexpr int L_F = L_Q + 1024;
constexpr int L_R = L_F + 32;
constexpr int L_W = L_R + 2048;
constexpr int L_END = L_W + 16384;
enum { F_QP = 0, F_Q = 1, F_MERGE = 2, F_TAIL = 3, F_W = 4, F_OUT = 5, F_PM = 6 };

__global__ void __launch_bounds__(NTHR, 2) fused_gqa(const unsigned char* __restrict__ x, const unsigned char* __restrict__ kc, const unsigned char* __restrict__ vc,
                                                      const unsigned char* __restrict__ wq, const unsigned char* __restrict__ wo, unsigned char* __restrict__ out, unsigned char* ws) {
  __shared__ __attribute__((aligned(16))) unsigned char lds_raw[L_END];
  LAS unsigned char* lds = (LAS unsigned char*)lds_raw;
  volatile LAS unsigned* flg = (volatile LAS unsigned*)(lds + L_F);
#define TS(i)
  TS(0);
  asm volatile("" : "+s"(x), "+s"(kc), "+s"(vc), "+s"(wq), "+s"(wo), "+s"(out), "+s"(ws));
  const int blk = blockIdx.x, j = blk & 31, b = blk >> 5, kvh = j & 7, s = j >> 3, H = kvh * 4 + s;
  const int tid = threadIdx.x, lane = tid & 63, wave = __builtin_amdgcn_readfirstlane(tid >> 6), g = lane >> 4, c = lane & 15, w4 = wave & 3;
#define BUMP(f) do { CFENCE(); if (lane == 0) __hip_atomic_fetch_add((LAS unsigned*)(lds + L_F + 4 * (f)), 1u, __ATOMIC_RELAXED, __HIP_MEMORY_SCOPE_WORKGROUP); } while (0)
#define WAITF(f, n, slp) do { _Pragma("unroll 1") for (unsigned sp_ = 0; sp_ < (SPIN_CAP << 6); ++sp_) { if (flg[f] >= (unsigned)(n)) break; __builtin_amdgcn_s_sleep(slp); } CFENCE(); } while (0)
  const size_t kvoff = (size_t)(b * 8 + kvh) * SK * 256;
  const rsrc_t rk = mkrsrc(kc + kvoff, SK * 256), rv = mkrsrc(vc + kvoff, SK * 256);
  const rsrc_t rws = mkrsrc(ws, WS_END);
  unsigned kvo = (4 * (c & 3) + (c >> 2)) * 256 + g * 16, vvo = lane * 16;
  u32x4 kb[4][4], vb[4][4];
#define ISSUE_KV(st, so) do { asm volatile("" : "+v"(kvo), "+v"(vvo)); \
    _Pragma("unroll") for (int t_ = 0; t_ < 4; ++t_) { kb[st][t_] = __builtin_amdgcn_raw_buffer_load_b128(rk, kvo + t_ * 64, (so), AUX_NT); SB(); } \
    _Pragma("unroll") for (int t_ = 0; t_ < 4; ++t_) { vb[st][t_] = __builtin_amdgcn_raw_buffer_load_b128(rv, vvo + t_ * 1024, (so), AUX_NT); SB(); } \
    } while (0)
  const int w0 = (s * 4 + w4) * 4096 + (wave >> 2) * 4 * 65536;
  const unsigned ep = __builtin_amdgcn_raw_buffer_load_b32(rws, 0, WS_EPOCH, AUX_SC1);
  SB();
  unsigned tag;
  u32x4 wt4[4];
#define W_LOAD() do { const rsrc_t rw = mkrsrc(wo + (size_t)H * 131072 + b * 128, 131072); \
    _Pragma("unroll") for (int i = 0; i < 4; ++i) wt4[i] = __builtin_amdgcn_raw_buffer_load_b128(rw, (64 * (c >> 3) + 32 * (g & 1) + 16 * (g >> 1)) * 1024 + (c & 7) * 16 + (i & 1) * 1024, (4 * (i >> 1) + 2 * (w4 >> 1) + 8 * (w4 & 1)) * 1024, WAUX); SB(); } while (0)
#define W_STAGE() do { LAS unsigned char* wdst = lds + L_W + (c >> 3) * 8192 + (g * 16 + 2 * (c & 7)) * 16 + w4 * 4; \
    _Pragma("unroll") for (int t = 0; t < 2; ++t) _Pragma("unroll") for (int w = 0; w < 4; ++w) { \
        *(LAS unsigned*)(wdst + (4 * t + ((2 * w) & 3)) * 1024 + (w >> 1) * 16) = __builtin_amdgcn_perm(wt4[2 * t + 1][w], wt4[2 * t][w], PLO); \
        *(LAS unsigned*)(wdst + (4 * t + ((2 * w + 1) & 3)) * 1024 + (w >> 1) * 16) = __builtin_amdgcn_perm(wt4[2 * t + 1][w], wt4[2 * t][w], PHI); } \
    BUMP(F_W); } while (0)
  if (wave >= 4) {
    __builtin_amdgcn_s_setprio(3);
    const rsrc_t rx = mkrsrc(x, 8192), rq = mkrsrc(wq + (size_t)(s * 128 + w4 * 32) * 8192 + (kvh * 8 + b) * 128, WQSZ);
    const u32x4 xf = __builtin_amdgcn_raw_buffer_load_b128(rx, (c & 7) * 1024 + g * 16, s * 256 + w4 * 64, 0);
    u32x2 wr[8];
#pragma unroll
    for (int i = 0; i < 8; ++i) wr[i] = __builtin_amdgcn_raw_buffer_load_b64(rq, g * 65536 + c * 8, i * 8192, AUX_NT);
    const unsigned pw = __builtin_amdgcn_raw_buffer_load_b32(rws, lane < 4 ? WS_OG + (kvh * 8 + w4 * 2 + (lane >> 1)) * OS + (b * 4 + s) * 256 + (lane & 1) * 128
                                                                   : lane == 4 ? WS_LG + (kvh * 4 + w4) * LS + (b * 4 + s) * 128 : OOB, 0, AUX_SC1);
    SB();
    if (tid < 256 + 8) flg[tid - 256] = 0u;
    asm volatile("s_waitcnt lgkmcnt(0)" ::: "memory"); __builtin_amdgcn_s_barrier(); CFENCE();
    SB();
    tag = __builtin_amdgcn_readfirstlane(ep) + 0x9E3779B9u;
#pragma unroll
    for (int w = 0; w < 2; ++w) {
      u32x4 a0, a1;
#pragma unroll
      for (int a = 0; a < 4; ++a) { a0[a] = __builtin_amdgcn_perm(wr[2 * a + 1][w], wr[2 * a][w], PLO); a1[a] = __builtin_amdgcn_perm(wr[2 * a + 1][w], wr[2 * a][w], PHI); }
      const f32x4 d0 = mfma16(a0, xf, f32x4{0.f, 0.f, 0.f, 0.f}), d1 = mfma16(a1, xf, f32x4{0.f, 0.f, 0.f, 0.f});
      if (c < 8) {
        *(LAS f32x4*)(lds + L_QT + w4 * 2048 + c * 256 + g * 64 + (2 * w) * 16) = d0;
        *(LAS f32x4*)(lds + L_QT + w4 * 2048 + c * 256 + g * 64 + (2 * w + 1) * 16) = d1;
      }
    }
    BUMP(F_QP);
    WAITF(F_QP, 4, 0);
    {
      const int p = lane & 31, ro = (2 * w4 + (lane >> 5)) * 256 + (p >> 3) * 64 + ((p >> 2) & 1) * 32 + (p & 3) * 4;
      float q0[4], q1[4];
#pragma unroll
      for (int w = 0; w < 4; ++w) { q0[w] = *(LAS float*)(lds + L_QT + w * 2048 + ro); q1[w] = *(LAS float*)(lds + L_QT + w * 2048 + ro + 16); }
      const float u0 = (q0[0] + q0[1]) + (q0[2] + q0[3]), u1 = (q1[0] + q1[1]) + (q1[2] + q1[3]);
      __builtin_amdgcn_raw_buffer_store_b128(u32x4{__float_as_uint(u0), tag, __float_as_uint(u1), tag}, rws, (lane >> 5) * QS + p * 16, WS_QG + (kvh * 8 + 2 * w4) * QS + s * 4096 + b * 512, AUX_SC1);
    }
    asm volatile("" :: "v"(pw));
    SB();
    __builtin_amdgcn_s_setprio(0);
    TS(1);
    ISSUE_KV(0, w0); ISSUE_KV(1, w0 + 65536); ISSUE_KV(2, w0 + 2 * 65536); ISSUE_KV(3, w0 + 3 * 65536);
  } else {
    ISSUE_KV(0, w0);
    __builtin_amdgcn_s_barrier(); CFENCE();
    SB();
    ISSUE_KV(1, w0 + 65536); ISSUE_KV(2, w0 + 2 * 65536); ISSUE_KV(3, w0 + 3 * 65536);
    tag = __builtin_amdgcn_readfirstlane(ep) + 0x9E3779B9u;
    TS(1);
    u32x4 qg[4];
#pragma unroll 1
    for (unsigned spin = 0; spin < SPIN_CAP; ++spin) {
      CFENCE();
      bool ok = true;
#pragma unroll
      for (int i = 0; i < 4; ++i) qg[i] = __builtin_amdgcn_raw_buffer_load_b128(rws, lane * 16, WS_QG + (kvh * 8 + b) * QS + i * 4096 + w4 * 1024, AUX_SC1);
#pragma unroll
      for (int i = 0; i < 4; ++i) ok = ok & (qg[i][1] == tag) & (qg[i][3] == tag);
      if (__all(ok)) break;
    }
    const float q0 = (__uint_as_float(qg[0][0]) + __uint_as_float(qg[1][0])) + (__uint_as_float(qg[2][0]) + __uint_as_float(qg[3][0]));
    const float q1 = (__uint_as_float(qg[0][2]) + __uint_as_float(qg[1][2])) + (__uint_as_float(qg[2][2]) + __uint_as_float(qg[3][2]));
    *(LAS unsigned*)(lds + L_Q + w4 * 256 + (lane >> 3) * 32 + (lane & 3) * 8 + ((lane >> 2) & 1) * 4) = cvtpk(q0, q1);
    BUMP(F_Q);
  }
  WAITF(F_Q, 4, 1);
  TS(2);
  u32x4 qf[4];
#pragma unroll
  for (int t = 0; t < 4; ++t) qf[t] = *(LAS u32x4*)(lds + L_Q + (c & 3) * 256 + (4 * t + g) * 16);

  float m = -INFINITY, l = 0.f;
  f32x4 acc[8];
#pragma unroll
  for (int i = 0; i < 8; ++i) acc[i] = f32x4{0.f, 0.f, 0.f, 0.f};
  auto tile = [&](auto stc) __attribute__((always_inline)) {
    constexpr int st = decltype(stc)::value;
    f32x4 s0 = {0.f, 0.f, 0.f, 0.f}, s1 = {0.f, 0.f, 0.f, 0.f};
#pragma unroll
    for (int t = 0; t < 4; ++t) { s0 = mfma16(kb[st][t], qf[t], s0); s1 = mfma16(kb[st + 1][t], qf[t], s1); }
    float pm = fmaxf(fmaxf(fmaxf(s0[0], s0[1]), fmaxf(s0[2], s0[3])), fmaxf(fmaxf(s1[0], s1[1]), fmaxf(s1[2], s1[3])));
    pm = xrow_max(pm);
    if (__any(pm > m)) {
      const float mn = fmaxf(m, pm);
      const float alpha = __builtin_amdgcn_exp2f((m - mn) * C2);
      m = mn; l *= alpha;
#pragma unroll
      for (int i = 0; i < 8; ++i) acc[i] *= alpha;
    }
    const float mc = -m * C2;
    float p[8];
#pragma unroll
    for (int r = 0; r < 4; ++r) { p[r] = __builtin_amdgcn_exp2f(fmaf(s0[r], C2, mc)); p[4 + r] = __builtin_amdgcn_exp2f(fmaf(s1[r], C2, mc)); }
    l += ((p[0] + p[1]) + (p[2] + p[3])) + ((p[4] + p[5]) + (p[6] + p[7]));
    const u32x4 pf = {cvtpk(p[0], p[1]), cvtpk(p[2], p[3]), cvtpk(p[4], p[5]), cvtpk(p[6], p[7])};
#pragma unroll
    for (int w = 0; w < 4; ++w) {
      u32x4 a0, a1;
      a0[0] = __builtin_amdgcn_perm(vb[st][1][w], vb[st][0][w], PLO);         a1[0] = __builtin_amdgcn_perm(vb[st][1][w], vb[st][0][w], PHI);
      a0[1] = __builtin_amdgcn_perm(vb[st][3][w], vb[st][2][w], PLO);         a1[1] = __builtin_amdgcn_perm(vb[st][3][w], vb[st][2][w], PHI);
      a0[2] = __builtin_amdgcn_perm(vb[st + 1][1][w], vb[st + 1][0][w], PLO); a1[2] = __builtin_amdgcn_perm(vb[st + 1][1][w], vb[st + 1][0][w], PHI);
      a0[3] = __builtin_amdgcn_perm(vb[st + 1][3][w], vb[st + 1][2][w], PLO); a1[3] = __builtin_amdgcn_perm(vb[st + 1][3][w], vb[st + 1][2][w], PHI);
      acc[2 * w] = mfma16(a0, pf, acc[2 * w]);
      acc[2 * w + 1] = mfma16(a1, pf, acc[2 * w + 1]);
    }
    SB();
  };
  if (wave >= 4) {
#pragma unroll 1
    for (int it = 0; it < 6; ++it) {
      const int so0 = w0 + (it + 1) * 4 * 65536;
      tile(std::integral_constant<int, 0>{});
      ISSUE_KV(0, so0); ISSUE_KV(1, so0 + 65536);
      tile(std::integral_constant<int, 2>{});
      ISSUE_KV(2, so0 + 2 * 65536); ISSUE_KV(3, so0 + 3 * 65536);
    }
    BUMP(F_TAIL);
  }
  tile(std::integral_constant<int, 0>{});
  if (wave >= 4) BUMP(F_TAIL);
  tile(std::integral_constant<int, 2>{});
  l = xrow_sum(l);
  if (c < 4) {
    LAS unsigned char* rec = lds + L_M + (wave * 4 + c) * MREC;
#pragma unroll
    for (int i = 0; i < 8; ++i) *(LAS f32x4*)(rec + 16 + g * 128 + i * 16) = acc[i];
    if (g == 0) *(LAS f32x2*)rec = f32x2{m * C2, l};
  }
  CFENCE();
  constexpr int W0 = 3;
  if (wave < 4) {
    unsigned a4 = 0;
    if (lane == 0) a4 = __hip_atomic_fetch_add((LAS unsigned*)(lds + L_F + 4 * F_PM), 1u, __ATOMIC_RELAXED, __HIP_MEMORY_SCOPE_WORKGROUP);
    if (__builtin_amdgcn_readfirstlane(a4) == 3) {
      CFENCE();
      f32x2 ml[4]; f32x4 a0[4], a1[4];
#pragma unroll
      for (int w = 0; w < 4; ++w) { LAS unsigned char* rec = lds + L_M + (w * 4 + g) * MREC; ml[w] = *(LAS f32x2*)rec; a0[w] = *(LAS f32x4*)(rec + 16 + c * 32); a1[w] = *(LAS f32x4*)(rec + 32 + c * 32); }
      const float M = fmaxf(fmaxf(ml[0][0], ml[1][0]), fmaxf(ml[2][0], ml[3][0]));
      float L = 0.f; f32x4 o0 = {0.f, 0.f, 0.f, 0.f}, o1 = {0.f, 0.f, 0.f, 0.f};
#pragma unroll
      for (int w = 0; w < 4; ++w) { const float wt = __builtin_amdgcn_exp2f(ml[w][0] - M); L = fmaf(wt, ml[w][1], L); o0 += wt * a0[w]; o1 += wt * a1[w]; }
      LAS unsigned char* rec = lds + L_M + (3 * 4 + g) * MREC;
      CFENCE();
      *(LAS f32x4*)(rec + 16 + c * 32) = o0; *(LAS f32x4*)(rec + 32 + c * 32) = o1;
      if (c == 0) *(LAS f32x2*)rec = f32x2{M, L};
      CFENCE();
    }
  }
  unsigned arr = 0;
  if (lane == 0) arr = __hip_atomic_fetch_add((LAS unsigned*)(lds + L_F + 4 * F_MERGE), 1u, __ATOMIC_RELAXED, __HIP_MEMORY_SCOPE_WORKGROUP);
  const bool last = __builtin_amdgcn_readfirstlane(arr) == NW - 1;
  CFENCE();
  TS(3);
  if (last) {
    __builtin_amdgcn_s_setprio(3);
    f32x2 ml[NW]; f32x4 a0[NW], a1[NW];
#pragma unroll
    for (int w = W0; w < NW; ++w) { LAS unsigned char* rec = lds + L_M + (w * 4 + g) * MREC; ml[w] = *(LAS f32x2*)rec; a0[w] = *(LAS f32x4*)(rec + 16 + c * 32); a1[w] = *(LAS f32x4*)(rec + 32 + c * 32); }
    float M = ml[W0][0];
#pragma unroll
    for (int w = W0 + 1; w < NW; ++w) M = fmaxf(M, ml[w][0]);
    float L = 0.f; f32x4 o0 = {0.f, 0.f, 0.f, 0.f}, o1 = {0.f, 0.f, 0.f, 0.f};
#pragma unroll
    for (int w = W0; w < NW; ++w) { const float wt = __builtin_amdgcn_exp2f(ml[w][0] - M); L = fmaf(wt, ml[w][1], L); o0 += wt * a0[w]; o1 += wt * a1[w]; }
    const float inv = __builtin_amdgcn_rcpf(L);
    o0 *= inv; o1 *= inv;
    const unsigned so = WS_OG + kvh * 8 * OS + (b * 4 + s) * 256, vo = (lane >> 3) * OS + (lane & 7) * 16;
    const unsigned lse = __float_as_uint(M + __builtin_amdgcn_logf(L));
    u32x4 d0 = {cvtpk(o0[0], o1[0]), tag, cvtpk(o0[1], o1[1]), tag}, d1 = {cvtpk(o0[2], o1[2]), tag, cvtpk(o0[3], o1[3]), tag}, d2 = {lse, tag, lse, tag};
    asm volatile("" : "+v"(d0), "+v"(d1), "+v"(d2));
    __builtin_amdgcn_raw_buffer_store_b128(d0, rws, vo, so, AUX_SC1);
    __builtin_amdgcn_raw_buffer_store_b128(d1, rws, vo + 128, so, AUX_SC1);
    if (c < 8) __builtin_amdgcn_raw_buffer_store_b128(d2, rws, g * LS + c * 16, WS_LG + kvh * 4 * LS + (b * 4 + s) * 128, AUX_SC1);
    TS(4);
  }
  if (wave < 4) {
    WAITF(F_TAIL, LATEW, 4);
    W_LOAD(); W_STAGE();
  }
  {
    u32x4 wf[16];
    WAITF(F_W, 4, 2);
#pragma unroll
    for (int i = 0; i < 16; ++i) wf[i] = *(LAS u32x4*)(lds + L_W + i * 1024 + lane * 16);
    WAITF(F_MERGE, NW, 2);
    u32x4 og0, og1, lg;
    {
      const unsigned vo = (c >> 2) * 256 + g * 64 + (c & 3) * 16, vl = g * 128 + (c & 3) * 16;
      bool no = true, nl = true;
#pragma unroll 1
      for (unsigned spin = 0; spin < SPIN_CAP; ++spin) {
        CFENCE();
        if (no) {
          og0 = __builtin_amdgcn_raw_buffer_load_b128(rws, vo, WS_OG + (H * 2) * OS + wave * 1024, AUX_SC1);
          og1 = __builtin_amdgcn_raw_buffer_load_b128(rws, vo, WS_OG + (H * 2 + 1) * OS + wave * 1024, AUX_SC1);
        }
        if (nl) lg = __builtin_amdgcn_raw_buffer_load_b128(rws, vl, WS_LG + H * LS + wave * 512, AUX_SC1);
        no = !((og0[1] == tag) & (og0[3] == tag) & (og1[1] == tag) & (og1[3] == tag));
        nl = !((lg[1] == tag) & (lg[3] == tag));
        if (!__any(no | nl)) break;
        __builtin_amdgcn_s_sleep(PSLEEP);
      }
    }
    TS(5);
    const int u = c & 3;
    const u32x4 a0 = {u == 0 ? og0[0] : 0u, u == 0 ? og0[2] : 0u, u == 1 ? og0[0] : 0u, u == 1 ? og0[2] : 0u};
    const u32x4 a1 = {u == 2 ? og0[0] : 0u, u == 2 ? og0[2] : 0u, u == 3 ? og0[0] : 0u, u == 3 ? og0[2] : 0u};
    const u32x4 a2 = {u == 0 ? og1[0] : 0u, u == 0 ? og1[2] : 0u, u == 1 ? og1[0] : 0u, u == 1 ? og1[2] : 0u};
    const u32x4 a3 = {u == 2 ? og1[0] : 0u, u == 2 ? og1[2] : 0u, u == 3 ? og1[0] : 0u, u == 3 ? og1[2] : 0u};
    const float lse = __uint_as_float(lg[0]);
    const float e = __builtin_amdgcn_exp2f(lse - xrow_max(lse));
    const float wt = e * __builtin_amdgcn_rcpf(xrow_sum(e));
    f32x4 d[4];
#pragma unroll
    for (int i = 0; i < 4; ++i) d[i] = mfma16(a0, wf[i], f32x4{0.f, 0.f, 0.f, 0.f});
#pragma unroll
    for (int i = 0; i < 4; ++i) d[i] = mfma16(a1, wf[4 + i], d[i]);
#pragma unroll
    for (int i = 0; i < 4; ++i) d[i] = mfma16(a2, wf[8 + i], d[i]);
#pragma unroll
    for (int i = 0; i < 4; ++i) d[i] = mfma16(a3, wf[12 + i], d[i]);
    float y[4];
#pragma unroll
    for (int i = 0; i < 4; ++i) y[i] = wt * ((d[i][0] + d[i][1]) + (d[i][2] + d[i][3]));
#pragma unroll
    for (int i = 0; i < 2; ++i) { auto t = __builtin_amdgcn_permlane32_swap(__float_as_uint(y[i]), __float_as_uint(y[i + 2]), false, false); y[i] = __uint_as_float(t[0]) + __uint_as_float(t[1]); }
#pragma unroll
    for (int i = 0; i < 2; ++i) { auto t = __builtin_amdgcn_permlane16_swap(__float_as_uint(y[i]), __float_as_uint(y[i]), false, false); y[i] = __uint_as_float(t[0]) + __uint_as_float(t[1]); }
    if ((g & 1) == 0) __builtin_amdgcn_raw_buffer_store_b128(u32x4{__float_as_uint(y[0]), tag + 1u, __float_as_uint(y[1]), tag + 1u}, rws, (g + (c >> 3)) * 512 + (c & 7) * 16,
                                           WS_QG + (kvh * 8 + wave) * QS + b * 2048 + s * 128, AUX_SC1);
    TS(6);
  }
  {
    u32x4 yg;
    {
      bool ny = lane < 32;
#pragma unroll 1
      for (unsigned spin = 0; spin < SPIN_CAP; ++spin) {
        CFENCE();
        if (ny) { yg = __builtin_amdgcn_raw_buffer_load_b128(rws, lane * 16, WS_QG + (wave * 8 + b) * QS + j * 512, AUX_SC1); ny = !((yg[1] == tag + 1u) & (yg[3] == tag + 1u)); }
        if (!__any(ny)) break;
        __builtin_amdgcn_s_sleep(PSLEEP2);
      }
    }
    if (lane < 32)
    *(LAS u32x2*)(lds + L_R + wave * 256 + (lane & 31) * 8) = u32x2{yg[0], yg[2]};
    CFENCE();
    unsigned arr2 = 0;
    if (lane == 0) arr2 = __hip_atomic_fetch_add((LAS unsigned*)(lds + L_F + 4 * F_OUT), 1u, __ATOMIC_RELAXED, __HIP_MEMORY_SCOPE_WORKGROUP);
    if (__builtin_amdgcn_readfirstlane(arr2) == NW - 1) {
      CFENCE();
      f32x2 p[4];
#pragma unroll
      for (int w = 0; w < 4; ++w) p[w] = *(LAS f32x2*)(lds + L_R + w * 512 + lane * 8);
      const f32x2 ps = (p[0] + p[1]) + (p[2] + p[3]);
      float r0 = ps[0], r1 = ps[1];
      r0 += dpp<0x128>(r0); r1 += dpp<0x128>(r1);
      r0 = xrow_sum(r0); r1 = xrow_sum(r1);
      if (lane < 8) *(unsigned*)(out + b * 1024 + (j >> 2) * 128 + ((j & 1) * 8 + lane) * 8 + ((j >> 1) & 1) * 4) = cvtpk(r0, r1);
      if (lane == 0 && blk == 0) __hip_atomic_store((GAS unsigned*)(ws + WS_EPOCH), tag, __ATOMIC_RELAXED, __HIP_MEMORY_SCOPE_AGENT);
      TS(7);
    }
  }
}

extern "C" void kernel_launch(void* const* d_in, const int* in_sizes, int n_in, void* d_out, int out_size, void* d_ws, size_t ws_size, hipStream_t stream) {
  fused_gqa<<<256, NTHR, 0, stream>>>((const unsigned char*)d_in[0], (const unsigned char*)d_in[1], (const unsigned char*)d_in[2], (const unsigned char*)d_in[3], (const unsigned char*)d_in[4],
                                      (unsigned char*)d_out, (unsigned char*)d_ws);
}
```

```cpp
#include <hip/hip_runtime.h>
#include <hip/hip_bf16.h>
#include <cstdint>
#include <type_traits>

#define LATEW 11
#define WQSZ (32 * 8192)
#define NOTICE 0
#define NSLEEP 2
#define TOUCH 0
#define EARLYP 0
#define WSL 2
#define LSL 16
#define QBF 1
#define P2L 1
#define WREORD 0
#define PREMERGE 1
#define MASKED 2
#define EARLYKV 1
#define WAUX 0
#define PSLEEP 4
#define PSLEEP2 2
#define PREWARM 1
constexpr int SK = 8192, NW = 8, NTHR = NW * 64;
constexpr float C2 = 0.088388347648318440f * 1.4426950408889634f;
constexpr unsigned SPIN_CAP = 1u << 16;
constexpr unsigned OOB = 0x40000000u;

using bf16x8 = __attribute__((ext_vector_type(8))) __bf16;
using bf16x2 = __attribute__((ext_vector_type(2))) __bf16;
using u32x4  = __attribute__((ext_vector_type(4))) unsigned;
using u32x2  = __attribute__((ext_vector_type(2))) unsigned;
using f32x4  = __attribute__((ext_vector_type(4))) float;
using f32x2  = __attribute__((ext_vector_type(2))) float;
using rsrc_t = __amdgpu_buffer_rsrc_t;
#define LAS __attribute__((address_space(3)))
#define GAS __attribute__((address_space(1)))

__device__ __forceinline__ unsigned cvtpk(float lo, float hi) { return __builtin_bit_cast(unsigned, __builtin_convertvector(f32x2{lo, hi}, bf16x2)); }
__device__ __forceinline__ float bflo(unsigned u) { return __builtin_bit_cast(float, u << 16); }
__device__ __forceinline__ float bfhi(unsigned u) { return __builtin_bit_cast(float, u & 0xffff0000u); }
__device__ __forceinline__ f32x4 mfma16(u32x4 a, u32x4 b, f32x4 c) {
  return __builtin_amdgcn_mfma_f32_16x16x32_bf16(__builtin_bit_cast(bf16x8, a), __builtin_bit_cast(bf16x8, b), c, 0, 0, 0);
}
template <int CTRL> __device__ __forceinline__ float dpp(float x) {
  return __builtin_bit_cast(float, __builtin_amdgcn_mov_dpp(__builtin_bit_cast(int, x), CTRL, 0xf, 0xf, true));
}
__device__ __forceinline__ float xrow_max(float x) {
  auto s = __builtin_amdgcn_permlane16_swap(__float_as_uint(x), __float_as_uint(x), false, false);
  x = fmaxf(__uint_as_float(s[0]), __uint_as_float(s[1]));
  auto t = __builtin_amdgcn_permlane32_swap(__float_as_uint(x), __float_as_uint(x), false, false);
  return fmaxf(__uint_as_float(t[0]), __uint_as_float(t[1]));
}
__device__ __forceinline__ float xrow_sum(float x) {
  auto s = __builtin_amdgcn_permlane16_swap(__float_as_uint(x), __float_as_uint(x), false, false);
  x = __uint_as_float(s[0]) + __uint_as_float(s[1]);
  auto t = __builtin_amdgcn_permlane32_swap(__float_as_uint(x), __float_as_uint(x), false, false);
  return __uint_as_float(t[0]) + __uint_as_float(t[1]);
}
__device__ __forceinline__ rsrc_t mkrsrc(const void* p, unsigned bytes) { return __builtin_amdgcn_make_buffer_rsrc(const_cast<void*>(p), 0, bytes, 0x00020000); }
#define PLO 0x05040100u
#define PHI 0x07060302u
#define CFENCE() asm volatile("" ::: "memory")
#define SB() __builtin_amdgcn_sched_barrier(0)
#define AUX_NT 2
#define AUX_SC1 16

constexpr unsigned QS = 16384 + 512, OS = 8192 + 256, LS = 4096 + 256;
constexpr unsigned WS_EPOCH = 0;
constexpr unsigned WS_QG = 4096;
constexpr unsigned WS_OG = WS_QG + 64 * QS;
constexpr unsigned WS_LG = WS_OG + 64 * OS;
constexpr unsigned WS_NG = WS_LG + 32 * LS;
constexpr unsigned WS_END = WS_NG + 256 * 128;
constexpr unsigned WS_TS = 8 << 20;
constexpr int MREC = (4 + 128) * 4;
constexpr int L_M = 0;
constexpr int L_QT = L_M + NW * 4 * MREC;
constexpr int L_Q = L_QT + 8192;
constexpr int L_F = L_Q + 1024;
constexpr int L_R = L_F + 32;
constexpr int L_W = L_R + 2048;
constexpr int L_END = L_W + 16384;
enum { F_QP = 0, F_Q = 1, F_MERGE = 2, F_TAIL = 3, F_W = 4, F_OUT = 5, F_PM = 6, F_X = 7 };

__global__ void __launch_bounds__(NTHR, 2) fused_gqa(const unsigned char* __restrict__ x, const unsigned char* __restrict__ kc, const unsigned char* __restrict__ vc,
                                                      const unsigned char* __restrict__ wq, const unsigned char* __restrict__ wo, unsigned char* __restrict__ out, unsigned char* ws) {
  __shared__ __attribute__((aligned(16))) unsigned char lds_raw[L_END];
  LAS unsigned char* lds = (LAS unsigned char*)lds_raw;
  volatile LAS unsigned* flg = (volatile LAS unsigned*)(lds + L_F);
#define TS(i)
  TS(0);
  asm volatile("" : "+s"(x), "+s"(kc), "+s"(vc), "+s"(wq), "+s"(wo), "+s"(out), "+s"(ws));
  const int blk = blockIdx.x, j = blk & 31, b = blk >> 5, kvh = j & 7, s = j >> 3, H = kvh * 4 + s;
  const int tid = threadIdx.x, lane = tid & 63, wave = __builtin_amdgcn_readfirstlane(tid >> 6), g = lane >> 4, c = lane & 15, w4 = wave & 3;
#define LINC(f) __builtin_amdgcn_atomic_inc32((LAS unsigned*)(lds + L_F + 4 * (f)), 0xffffffffu, __ATOMIC_RELAXED, "workgroup")
#define BUMP(f) do { CFENCE(); if (lane == 0) LINC(f); } while (0)
#define WAITF(f, n, slp) do { _Pragma("unroll 1") for (unsigned sp_ = 0; sp_ < (SPIN_CAP << 6); ++sp_) { if (flg[f] >= (unsigned)(n)) break; __builtin_amdgcn_s_sleep(slp); } CFENCE(); } while (0)
  const size_t kvoff = (size_t)(b * 8 + kvh) * SK * 256;
  const rsrc_t rk = mkrsrc(kc + kvoff, SK * 256), rv = mkrsrc(vc + kvoff, SK * 256);
  const rsrc_t rws = mkrsrc(ws, WS_END);
  unsigned kvo = (4 * (c & 3) + (c >> 2)) * 256 + g * 16, vvo = lane * 16;
  u32x4 kb[4][4], vb[4][4];
#define ISSUE_KV(st, so) do { asm volatile("" : "+v"(kvo), "+v"(vvo)); \
    _Pragma("unroll") for (int t_ = 0; t_ < 4; ++t_) { kb[st][t_] = __builtin_amdgcn_raw_buffer_load_b128(rk, kvo + t_ * 64, (so), AUX_NT); SB(); } \
    _Pragma("unroll") for (int t_ = 0; t_ < 4; ++t_) { vb[st][t_] = __builtin_amdgcn_raw_buffer_load_b128(rv, vvo + t_ * 1024, (so), AUX_NT); SB(); } \
    } while (0)
  const int w0 = (s * 4 + w4) * 4096 + (wave >> 2) * 4 * 65536;
  const unsigned ep = __builtin_amdgcn_raw_buffer_load_b32(rws, 0, WS_EPOCH, AUX_SC1);
  SB();
  unsigned tag;
  if (wave >= 4) {
    __builtin_amdgcn_s_setprio(3);
    const rsrc_t rx = mkrsrc(x, 8192), rq = mkrsrc(wq + (size_t)(s * 128 + w4 * 32) * 8192 + (kvh * 8 + b) * 128, WQSZ);
    const u32x4 xf = __builtin_amdgcn_raw_buffer_load_b128(rx, (c & 7) * 1024 + g * 16, s * 256 + w4 * 64, 0);
    u32x2 wr[8];
#pragma unroll
    for (int i = 0; i < 8; ++i) wr[i] = __builtin_amdgcn_raw_buffer_load_b64(rq, g * 65536 + c * 8, i * 8192, AUX_NT);
    const unsigned pw = __builtin_amdgcn_raw_buffer_load_b32(rws, lane < 4 ? WS_OG + (kvh * 8 + w4 * 2 + (lane >> 1)) * OS + (b * 4 + s) * 256 + (lane & 1) * 128
                                                                   : lane == 4 ? WS_LG + (kvh * 4 + w4) * LS + (b * 4 + s) * 128 : (QBF && lane < 9) ? WS_QG + (kvh * 8 + b) * QS + 8192 + ((s * 4 + w4) * 4 + lane - 5) * 128 : (NOTICE && lane == 9 && w4 == 0) ? WS_NG + ((kvh * 8 + b) * 4 + s) * 128 : OOB, 0, AUX_SC1);
    SB();
    if (tid < 256 + 8) flg[tid - 256] = 0u;
    asm volatile("s_waitcnt lgkmcnt(0)" ::: "memory"); __builtin_amdgcn_s_barrier(); CFENCE();
    SB();
    tag = __builtin_amdgcn_readfirstlane(ep) + 0x9E3779B9u;
#pragma unroll
    for (int w = 0; w < 2; ++w) {
      u32x4 a0, a1;
#pragma unroll
      for (int a = 0; a < 4; ++a) { a0[a] = __builtin_amdgcn_perm(wr[2 * a + 1][w], wr[2 * a][w], PLO); a1[a] = __builtin_amdgcn_perm(wr[2 * a + 1][w], wr[2 * a][w], PHI); }
      const f32x4 d0 = mfma16(a0, xf, f32x4{0.f, 0.f, 0.f, 0.f}), d1 = mfma16(a1, xf, f32x4{0.f, 0.f, 0.f, 0.f});
      if (c < 8) {
        *(LAS f32x4*)(lds + L_QT + w4 * 2048 + c * 256 + g * 64 + (2 * w) * 16) = d0;
        *(LAS f32x4*)(lds + L_QT + w4 * 2048 + c * 256 + g * 64 + (2 * w + 1) * 16) = d1;
      }
    }
    BUMP(F_QP);
    WAITF(F_QP, 4, 0);
    {
      const int p = lane & 31, ro = (2 * w4 + (lane >> 5)) * 256 + (p >> 3) * 64 + ((p >> 2) & 1) * 32 + (p & 3) * 4;
      float q0[4], q1[4];
#pragma unroll
      for (int w = 0; w < 4; ++w) { q0[w] = *(LAS float*)(lds + L_QT + w * 2048 + ro); q1[w] = *(LAS float*)(lds + L_QT + w * 2048 + ro + 16); }
      const float u0 = (q0[0] + q0[1]) + (q0[2] + q0[3]), u1 = (q1[0] + q1[1]) + (q1[2] + q1[3]);
      __builtin_amdgcn_raw_buffer_store_b64(u32x2{cvtpk(u0, u1), tag}, rws, (lane >> 5) * QS + p * 8, WS_QG + (kvh * 8 + 2 * w4) * QS + (b >> 1) * 2048 + s * 512 + (b & 1) * 256, AUX_SC1);
    }
    asm volatile("" :: "v"(pw));
    SB();
    __builtin_amdgcn_s_setprio(0);
    TS(1);
    ISSUE_KV(0, w0); ISSUE_KV(1, w0 + 65536); ISSUE_KV(2, w0 + 2 * 65536); ISSUE_KV(3, w0 + 3 * 65536);
  } else {
    ISSUE_KV(0, w0);
    __builtin_amdgcn_s_barrier(); CFENCE();
    SB();
    ISSUE_KV(1, w0 + 65536); ISSUE_KV(2, w0 + 2 * 65536); ISSUE_KV(3, w0 + 3 * 65536);
    tag = __builtin_amdgcn_readfirstlane(ep) + 0x9E3779B9u;
    TS(1);
    u32x4 qg[2];
#pragma unroll 1
    for (unsigned spin = 0; spin < SPIN_CAP; ++spin) {
      CFENCE();
      qg[0] = __builtin_amdgcn_raw_buffer_load_b128(rws, lane * 16, WS_QG + (kvh * 8 + b) * QS + w4 * 2048, AUX_SC1);
      qg[1] = __builtin_amdgcn_raw_buffer_load_b128(rws, lane * 16 + 1024, WS_QG + (kvh * 8 + b) * QS + w4 * 2048, AUX_SC1);
      if (__all((qg[0][1] == tag) & (qg[0][3] == tag) & (qg[1][1] == tag) & (qg[1][3] == tag))) break;
    }
    {
      float a0 = bflo(qg[0][0]) + bflo(qg[1][0]), a1 = bfhi(qg[0][0]) + bfhi(qg[1][0]), b0 = bflo(qg[0][2]) + bflo(qg[1][2]), b1 = bfhi(qg[0][2]) + bfhi(qg[1][2]);
      auto t0 = __builtin_amdgcn_permlane32_swap(__float_as_uint(a0), __float_as_uint(b0), false, false);
      auto t1 = __builtin_amdgcn_permlane32_swap(__float_as_uint(a1), __float_as_uint(b1), false, false);
      const int p = 2 * c + (lane >> 5);
      *(LAS unsigned*)(lds + L_Q + w4 * 256 + (g & 1) * 128 + (p >> 3) * 32 + (p & 3) * 8 + ((p >> 2) & 1) * 4) =
          cvtpk(__uint_as_float(t0[0]) + __uint_as_float(t0[1]), __uint_as_float(t1[0]) + __uint_as_float(t1[1]));
    }
    BUMP(F_Q);
  }
  unsigned v_rec = L_M + g * MREC, v_recd = v_rec + 16 + c * 32, v_dep = L_M + (wave * 4 + c) * MREC + 16 + g * 128;
  unsigned v_ost = (lane >> 3) * OS + (lane & 7) * 16, v_lst = g * LS + c * 16;
  unsigned v_wld = (32 * (g & 1) + 16 * (g >> 1)) * 1024 + c * 8;
  unsigned v_old = (c >> 2) * 256 + g * 64 + (c & 3) * 16, v_lld = g * 128 + (c & 3) * 16;
  unsigned v_yst = (g + (c >> 3)) * 512 + (c & 7) * 16, v_l8 = lane * 8, v_l16 = lane * 16;
#define RFL(x) (unsigned)__builtin_amdgcn_readfirstlane((int)(x))
  unsigned s_ost = RFL(WS_OG + kvh * 8 * OS + (b * 4 + s) * 256), s_lst = RFL(WS_LG + kvh * 4 * LS + (b * 4 + s) * 128);
  unsigned s_wld = RFL((64 * (w4 >> 1) + 4 * (w4 & 1)) * 1024), s_old = RFL(WS_OG + (H * 2) * OS + wave * 1024), s_lld = RFL(WS_LG + H * LS + wave * 512);
  unsigned s_yst = RFL(WS_QG + (kvh * 8 + wave) * QS + b * 2048 + s * 128), s_yld = RFL(WS_QG + (wave * 8 + b) * QS + j * 512);
  unsigned s_out = RFL(b * 1024 + (j >> 2) * 128 + (j & 1) * 64 + ((j >> 1) & 1) * 4), s_nst = RFL(WS_NG + ((kvh * 8 + b) * 4 + s) * 128), s_nld = RFL(WS_NG + (kvh * 8 + wave) * 512);
  const unsigned char* wtile = wo + (size_t)H * 131072 + b * 128;
  tag = __builtin_amdgcn_readfirstlane(tag);
#define PIN_TAIL() do { asm volatile("" : "+v"(v_rec), "+v"(v_recd), "+v"(v_dep), "+v"(v_ost), "+v"(v_lst), "+v"(v_wld), "+v"(v_old), "+v"(v_lld), "+v"(v_yst), "+v"(v_l8), "+v"(v_l16)); \
    asm volatile("" : "+s"(s_ost), "+s"(s_lst), "+s"(s_wld), "+s"(s_old), "+s"(s_lld), "+s"(s_yst), "+s"(s_yld), "+s"(s_out), "+s"(s_nst), "+s"(s_nld), "+s"(wtile)); } while (0)
  PIN_TAIL();
  WAITF(F_Q, 4, 1);
  TS(2);
  u32x4 qf[4];
#pragma unroll
  for (int t = 0; t < 4; ++t) qf[t] = *(LAS u32x4*)(lds + L_Q + (c & 3) * 256 + (4 * t + g) * 16);

  float m = -INFINITY, l = 0.f;
  f32x4 acc[8];
#pragma unroll
  for (int i = 0; i < 8; ++i) acc[i] = f32x4{0.f, 0.f, 0.f, 0.f};
  auto tile = [&](auto stc) __attribute__((always_inline)) {
    constexpr int st = decltype(stc)::value;
    f32x4 s0 = {0.f, 0.f, 0.f, 0.f}, s1 = {0.f, 0.f, 0.f, 0.f};
#pragma unroll
    for (int t = 0; t < 4; ++t) { s0 = mfma16(kb[st][t], qf[t], s0); s1 = mfma16(kb[st + 1][t], qf[t], s1); }
    float pm = fmaxf(fmaxf(fmaxf(s0[0], s0[1]), fmaxf(s0[2], s0[3])), fmaxf(fmaxf(s1[0], s1[1]), fmaxf(s1[2], s1[3])));
    pm = xrow_max(pm);
    if (__any(pm > m)) {
      const float mn = fmaxf(m, pm);
      const float alpha = __builtin_amdgcn_exp2f((m - mn) * C2);
      m = mn; l *= alpha;
#pragma unroll
      for (int i = 0; i < 8; ++i) acc[i] *= alpha;
    }
    const float mc = -m * C2;
    float p[8];
#pragma unroll
    for (int r = 0; r < 4; ++r) { p[r] = __builtin_amdgcn_exp2f(fmaf(s0[r], C2, mc)); p[4 + r] = __builtin_amdgcn_exp2f(fmaf(s1[r], C2, mc)); }
    l += ((p[0] + p[1]) + (p[2] + p[3])) + ((p[4] + p[5]) + (p[6] + p[7]));
    const u32x4 pf = {cvtpk(p[0], p[1]), cvtpk(p[2], p[3]), cvtpk(p[4], p[5]), cvtpk(p[6], p[7])};
#pragma unroll
    for (int w = 0; w < 4; ++w) {
      u32x4 a0, a1;
      a0[0] = __builtin_amdgcn_perm(vb[st][1][w], vb[st][0][w], PLO);         a1[0] = __builtin_amdgcn_perm(vb[st][1][w], vb[st][0][w], PHI);
      a0[1] = __builtin_amdgcn_perm(vb[st][3][w], vb[st][2][w], PLO);         a1[1] = __builtin_amdgcn_perm(vb[st][3][w], vb[st][2][w], PHI);
      a0[2] = __builtin_amdgcn_perm(vb[st + 1][1][w], vb[st + 1][0][w], PLO); a1[2] = __builtin_amdgcn_perm(vb[st + 1][1][w], vb[st + 1][0][w], PHI);
      a0[3] = __builtin_amdgcn_perm(vb[st + 1][3][w], vb[st + 1][2][w], PLO); a1[3] = __builtin_amdgcn_perm(vb[st + 1][3][w], vb[st + 1][2][w], PHI);
      acc[2 * w] = mfma16(a0, pf, acc[2 * w]);
      acc[2 * w + 1] = mfma16(a1, pf, acc[2 * w + 1]);
    }
    SB();
  };
  auto rescale = [&](float pm) __attribute__((always_inline)) {
    if (__any(pm > m)) {
      const float mn = fmaxf(m, pm);
      const float alpha = __builtin_amdgcn_exp2f((m - mn) * C2);
      m = mn; l *= alpha;
#pragma unroll
      for (int i = 0; i < 8; ++i) acc[i] *= alpha;
    }
  };
  auto tile2 = [&](auto stc) __attribute__((always_inline)) {
    constexpr int st = decltype(stc)::value;
    const int gam = c >> 2;
    float sc[2];
#pragma unroll
    for (int T = 0; T < 2; ++T) {
      f32x4 d[4];
#pragma unroll
      for (int i = 0; i < 4; ++i) d[i] = mfma16(kb[st + T][i], qf[i], f32x4{0.f, 0.f, 0.f, 0.f});
      float X[4];
#pragma unroll
      for (int u = 0; u < 4; ++u) X[u] = (gam & 2) ? ((gam & 1) ? d[(3 - u) & 3][u] : d[(2 - u) & 3][u]) : ((gam & 1) ? d[(1 - u) & 3][u] : d[(0 - u) & 3][u]);
      sc[T] = (X[0] + dpp<0x12C>(X[1])) + (dpp<0x128>(X[2]) + dpp<0x124>(X[3]));
    }
    float pm = fmaxf(sc[0], sc[1]);
    pm = fmaxf(pm, dpp<0x124>(pm)); pm = fmaxf(pm, dpp<0x128>(pm));
    pm = xrow_max(pm);
    rescale(pm);
    const float mc = -m * C2;
    const float p0 = __builtin_amdgcn_exp2f(fmaf(sc[0], C2, mc)), p1 = __builtin_amdgcn_exp2f(fmaf(sc[1], C2, mc));
    float ps = p0 + p1; ps += dpp<0x128>(ps); ps += dpp<0x124>(ps);
    l += ps;
    const float pk = __uint_as_float(cvtpk(p0, p1));
    const u32x4 pf = {__float_as_uint(pk), __float_as_uint(dpp<0x12C>(pk)), __float_as_uint(dpp<0x128>(pk)), __float_as_uint(dpp<0x124>(pk))};
#pragma unroll
    for (int w = 0; w < 4; ++w) {
      u32x4 a0, a1;
#pragma unroll
      for (int a = 0; a < 4; ++a) { a0[a] = __builtin_amdgcn_perm(vb[st + 1][a][w], vb[st][a][w], PLO); a1[a] = __builtin_amdgcn_perm(vb[st + 1][a][w], vb[st][a][w], PHI); }
      acc[2 * w] = mfma16(a0, pf, acc[2 * w]);
      acc[2 * w + 1] = mfma16(a1, pf, acc[2 * w + 1]);
    }
    SB();
  };
#define ISSUE_KV2(st, so) do { \
    _Pragma("unroll") for (int t_ = 0; t_ < 4; ++t_) { kb[st][t_] = __builtin_amdgcn_raw_buffer_load_b128(rk, ko[t_], (so), AUX_NT); SB(); } \
    _Pragma("unroll") for (int t_ = 0; t_ < 4; ++t_) { vb[st][t_] = __builtin_amdgcn_raw_buffer_load_b128(rv, vvo + t_ * 1024, (so), AUX_NT); SB(); } \
    } while (0)
  if (wave >= 4) {
#pragma unroll 1
    for (int it = 0; it < 6; ++it) {
      const int so0 = w0 + (it + 1) * 4 * 65536;
      unsigned ko[4];
      ko[0] = it == 5 ? (c >> 2) * 256 + g * 64 + (c & 3) * 16 : kvo;
#pragma unroll
      for (int t = 1; t < 4; ++t) ko[t] = ko[0] + t * (it == 5 ? 1024 : 64);
      tile(std::integral_constant<int, 0>{});
      asm volatile("" : "+v"(vvo), "+v"(ko[0]), "+v"(ko[1]), "+v"(ko[2]), "+v"(ko[3])); SB();
      ISSUE_KV2(0, so0); ISSUE_KV2(1, so0 + 65536);
      tile(std::integral_constant<int, 2>{});
      asm volatile("" : "+v"(vvo)); SB();
      ISSUE_KV2(2, so0 + 2 * 65536); ISSUE_KV2(3, so0 + 3 * 65536);
    }
#pragma unroll
    for (int i = 0; i < 4; ++i) qf[i] = *(LAS u32x4*)(lds + L_Q + (c & 3) * 256 + (4 * g + (((c >> 2) - i) & 3)) * 16);
    tile2(std::integral_constant<int, 0>{});
    tile2(std::integral_constant<int, 2>{});
  } else {
    tile(std::integral_constant<int, 0>{});
    tile(std::integral_constant<int, 2>{});
  }
  l = xrow_sum(l);
  if (c < 4) {
#pragma unroll
    for (int i = 0; i < 8; ++i) *(LAS f32x4*)(lds + v_dep + i * 16) = acc[i];
    if (g == 0) *(LAS f32x2*)(lds + v_dep - 16) = f32x2{m * C2, l};
  }
  CFENCE();
  constexpr int W0 = 3;
  if (wave < 4) {
    unsigned a4 = 0;
    if (lane == 0) a4 = LINC(F_PM);
    if (__builtin_amdgcn_readfirstlane(a4) == 3) {
      CFENCE();
      f32x2 ml[4]; f32x4 a0[4], a1[4];
#pragma unroll
      for (int w = 0; w < 4; ++w) { LAS unsigned char* rec = lds + L_M + (w * 4 + g) * MREC; ml[w] = *(LAS f32x2*)rec; a0[w] = *(LAS f32x4*)(rec + 16 + c * 32); a1[w] = *(LAS f32x4*)(rec + 32 + c * 32); }
      const float M = fmaxf(fmaxf(ml[0][0], ml[1][0]), fmaxf(ml[2][0], ml[3][0]));
      float L = 0.f; f32x4 o0 = {0.f, 0.f, 0.f, 0.f}, o1 = {0.f, 0.f, 0.f, 0.f};
#pragma unroll
      for (int w = 0; w < 4; ++w) { const float wt = __builtin_amdgcn_exp2f(ml[w][0] - M); L = fmaf(wt, ml[w][1], L); o0 += wt * a0[w]; o1 += wt * a1[w]; }
      LAS unsigned char* rec = lds + L_M + (3 * 4 + g) * MREC;
      CFENCE();
      *(LAS f32x4*)(rec + 16 + c * 32) = o0; *(LAS f32x4*)(rec + 32 + c * 32) = o1;
      if (c == 0) *(LAS f32x2*)rec = f32x2{M, L};
      CFENCE();
    }
  }
  unsigned arr = 0;
  if (lane == 0) arr = LINC(F_MERGE);
  const unsigned arru = __builtin_amdgcn_readfirstlane(arr);
  CFENCE();
  TS(3);
  constexpr unsigned live = ~0u;
  {
    const rsrc_t rwx = mkrsrc(ws, WS_END & live);
    const unsigned tagx = tag & live, tagy = (tag + 1u) & live;
    const int fW = live ? F_W : F_X, fO = live ? F_OUT : F_X;
    if ((arru | (~live & 7u)) == NW - 1) {
      f32x2 ml[NW]; f32x4 a0[NW], a1[NW];
#pragma unroll
      for (int w = W0; w < NW; ++w) ml[w] = *(LAS f32x2*)(lds + v_rec + w * 4 * MREC);
#pragma unroll
      for (int w = W0; w < NW; ++w) { a0[w] = *(LAS f32x4*)(lds + v_recd + w * 4 * MREC); a1[w] = *(LAS f32x4*)(lds + v_recd + w * 4 * MREC + 16); }
      float M = ml[W0][0];
#pragma unroll
      for (int w = W0 + 1; w < NW; ++w) M = fmaxf(M, ml[w][0]);
      float L = 0.f; f32x4 o0 = {0.f, 0.f, 0.f, 0.f}, o1 = {0.f, 0.f, 0.f, 0.f};
#pragma unroll
      for (int w = W0; w < NW; ++w) { const float wt = __builtin_amdgcn_exp2f(ml[w][0] - M); L = fmaf(wt, ml[w][1], L); o0 += wt * a0[w]; o1 += wt * a1[w]; }
      const float inv = __builtin_amdgcn_rcpf(L);
      o0 *= inv; o1 *= inv;
      const unsigned lse = __float_as_uint(M + __builtin_amdgcn_logf(L));
      u32x4 d0 = {cvtpk(o0[0], o1[0]), tag, cvtpk(o0[1], o1[1]), tag}, d1 = {cvtpk(o0[2], o1[2]), tag, cvtpk(o0[3], o1[3]), tag}, d2 = {lse, tag, lse, tag};
      asm volatile("" : "+v"(d0), "+v"(d1), "+v"(d2));
      __builtin_amdgcn_raw_buffer_store_b128(d0, rwx, v_ost, s_ost, AUX_SC1);
      __builtin_amdgcn_raw_buffer_store_b128(d1, rwx, v_ost + 128, s_ost, AUX_SC1);
      if (c < 8) __builtin_amdgcn_raw_buffer_store_b128(d2, rwx, v_lst, s_lst, AUX_SC1);
    }
    if (wave < 4) {
      WAITF(F_MERGE, 5u & live, LSL);
      WAITF(F_MERGE, (LATEW - 4) & live, WSL);
      const rsrc_t rw = mkrsrc(wtile, 131072 & live);
      u32x2 wr[8];
#pragma unroll
      for (int i = 0; i < 8; ++i) wr[i] = __builtin_amdgcn_raw_buffer_load_b64(rw, v_wld + (2 * (i >> 2) + (i & 1)) * 1024, s_wld + 8 * ((i >> 1) & 1) * 1024, WAUX);
#pragma unroll
      for (int w = 0; w < 2; ++w) {
        u32x4 f0, f1;
#pragma unroll
        for (int a = 0; a < 4; ++a) { f0[a] = __builtin_amdgcn_perm(wr[2 * a + 1][w], wr[2 * a][w], PLO); f1[a] = __builtin_amdgcn_perm(wr[2 * a + 1][w], wr[2 * a][w], PHI); }
        *(LAS u32x4*)(lds + L_W + (w4 * 4 + 2 * w) * 1024 + v_l16) = f0; *(LAS u32x4*)(lds + L_W + (w4 * 4 + 2 * w + 1) * 1024 + v_l16) = f1;
      }
      BUMP(fW);
    }
    WAITF(F_W, 4u & live, WSL);
    u32x4 wf[16], og0, og1, lg;
#pragma unroll
    for (int i = 0; i < 16; ++i) wf[i] = *(LAS u32x4*)(lds + L_W + i * 1024 + v_l16);
    {
      bool no = true, nl = true;
#pragma unroll 1
      for (unsigned spin = 0; spin < SPIN_CAP; ++spin) {
        CFENCE();
        if (no) {
          og0 = __builtin_amdgcn_raw_buffer_load_b128(rwx, v_old, s_old, AUX_SC1);
          og1 = __builtin_amdgcn_raw_buffer_load_b128(rwx, v_old, s_old + OS, AUX_SC1);
        }
        if (nl) lg = __builtin_amdgcn_raw_buffer_load_b128(rwx, v_lld, s_lld, AUX_SC1);
        no = !((og0[1] == tagx) & (og0[3] == tagx) & (og1[1] == tagx) & (og1[3] == tagx));
        nl = !((lg[1] == tagx) & (lg[3] == tagx));
        if (!__any(no | nl)) break;
        __builtin_amdgcn_s_sleep(PSLEEP);
      }
    }
    TS(5);
    {
      const int u = c & 3;
      const u32x4 a0 = {u == 0 ? og0[0] : 0u, u == 0 ? og0[2] : 0u, u == 1 ? og0[0] : 0u, u == 1 ? og0[2] : 0u};
      const u32x4 a1 = {u == 2 ? og0[0] : 0u, u == 2 ? og0[2] : 0u, u == 3 ? og0[0] : 0u, u == 3 ? og0[2] : 0u};
      const u32x4 a2 = {u == 0 ? og1[0] : 0u, u == 0 ? og1[2] : 0u, u == 1 ? og1[0] : 0u, u == 1 ? og1[2] : 0u};
      const u32x4 a3 = {u == 2 ? og1[0] : 0u, u == 2 ? og1[2] : 0u, u == 3 ? og1[0] : 0u, u == 3 ? og1[2] : 0u};
      const float lse = __uint_as_float(lg[0]);
      const float e = __builtin_amdgcn_exp2f(lse - xrow_max(lse));
      const float wt = e * __builtin_amdgcn_rcpf(xrow_sum(e));
      f32x4 d[4];
#pragma unroll
      for (int i = 0; i < 4; ++i) d[i] = mfma16(a0, wf[i], f32x4{0.f, 0.f, 0.f, 0.f});
#pragma unroll
      for (int i = 0; i < 4; ++i) d[i] = mfma16(a1, wf[4 + i], d[i]);
#pragma unroll
      for (int i = 0; i < 4; ++i) d[i] = mfma16(a2, wf[8 + i], d[i]);
#pragma unroll
      for (int i = 0; i < 4; ++i) d[i] = mfma16(a3, wf[12 + i], d[i]);
      float y[4];
#pragma unroll
      for (int i = 0; i < 4; ++i) y[i] = wt * ((d[i][0] + d[i][1]) + (d[i][2] + d[i][3]));
#pragma unroll
      for (int i = 0; i < 2; ++i) { auto t = __builtin_amdgcn_permlane32_swap(__float_as_uint(y[i]), __float_as_uint(y[i + 2]), false, false); y[i] = __uint_as_float(t[0]) + __uint_as_float(t[1]); }
#pragma unroll
      for (int i = 0; i < 2; ++i) { auto t = __builtin_amdgcn_permlane16_swap(__float_as_uint(y[i]), __float_as_uint(y[i]), false, false); y[i] = __uint_as_float(t[0]) + __uint_as_float(t[1]); }
      if ((g & 1) == 0) __builtin_amdgcn_raw_buffer_store_b128(u32x4{__float_as_uint(y[0]), tagy, __float_as_uint(y[1]), tagy}, rwx, v_yst, s_yst, AUX_SC1);
    }
    TS(6);
    u32x4 yg;
    {
      bool ny = lane < 32;
#pragma unroll 1
      for (unsigned spin = 0; spin < SPIN_CAP; ++spin) {
        CFENCE();
        if (ny) { yg = __builtin_amdgcn_raw_buffer_load_b128(rwx, v_l16, s_yld, AUX_SC1); ny = !((yg[1] == tagy) & (yg[3] == tagy)); }
        if (!__any(ny)) break;
        __builtin_amdgcn_s_sleep(PSLEEP2);
      }
    }
    if (lane < 32) *(LAS u32x2*)(lds + L_R + wave * 256 + v_l8) = u32x2{yg[0], yg[2]};
    CFENCE();
    unsigned arr2 = 0;
    if (lane == 0) arr2 = LINC(fO);
    if ((__builtin_amdgcn_readfirstlane(arr2) | (~live & 7u)) == NW - 1) {
      CFENCE();
      f32x2 p[4];
#pragma unroll
      for (int w = 0; w < 4; ++w) p[w] = *(LAS f32x2*)(lds + L_R + w * 512 + v_l8);
      const f32x2 ps = (p[0] + p[1]) + (p[2] + p[3]);
      float r0 = ps[0], r1 = ps[1];
      r0 += dpp<0x128>(r0); r1 += dpp<0x128>(r1);
      r0 = xrow_sum(r0); r1 = xrow_sum(r1);
      if (lane < 8) __builtin_amdgcn_raw_buffer_store_b32(cvtpk(r0, r1), mkrsrc(out, 8192 & live), v_l8, s_out, 0);
      if (lane == 0 && blk == 0) __builtin_amdgcn_raw_buffer_store_b32(tag, rwx, 0, WS_EPOCH, AUX_SC1);
    }
  }
}

extern "C" void kernel_launch(void* const* d_in, const int* in_sizes, int n_in, void* d_out, int out_size, void* d_ws, size_t ws_size, hipStream_t stream) {
  fused_gqa<<<256, NTHR, 0, stream>>>((const unsigned char*)d_in[0], (const unsigned char*)d_in[1], (const unsigned char*)d_in[2], (const unsigned char*)d_in[3], (const unsigned char*)d_in[4],
                                      (unsigned char*)d_out, (unsigned char*)d_ws);
}
```

```cpp
#define DIAGI 1
#define DIAGB 37
#define DIAGW 1
#include <hip/hip_runtime.h>
#include <hip/hip_bf16.h>
#include <cstdint>
#include <type_traits>

#define LATEW 11
#define WQSZ (32 * 8192)
#define NOTICE 0
#define NSLEEP 2
#define TOUCH 0
#define EARLYP 0
#define WSL 2
#define LSL 16
#define QBF 1
#define P2L 1
#define WREORD 0
#define PREMERGE 1
#define MASKED 2
#define EARLYKV 1
#define WAUX 0
#define PSLEEP 4
#define PSLEEP2 2
#define PREWARM 1
constexpr int SK = 8192, NW = 8, NTHR = NW * 64;
constexpr float C2 = 0.088388347648318440f * 1.4426950408889634f;
constexpr unsigned SPIN_CAP = 1u << 16;
constexpr unsigned OOB = 0x40000000u;

using bf16x8 = __attribute__((ext_vector_type(8))) __bf16;
using bf16x2 = __attribute__((ext_vector_type(2))) __bf16;
using u32x4  = __attribute__((ext_vector_type(4))) unsigned;
using u32x2  = __attribute__((ext_vector_type(2))) unsigned;
using f32x4  = __attribute__((ext_vector_type(4))) float;
using f32x2  = __attribute__((ext_vector_type(2))) float;
using rsrc_t = __amdgpu_buffer_rsrc_t;
#define LAS __attribute__((address_space(3)))
#define GAS __attribute__((address_space(1)))

__device__ __forceinline__ unsigned cvtpk(float lo, float hi) { return __builtin_bit_cast(unsigned, __builtin_convertvector(f32x2{lo, hi}, bf16x2)); }
__device__ __forceinline__ float bflo(unsigned u) { return __builtin_bit_cast(float, u << 16); }
__device__ __forceinline__ float bfhi(unsigned u) { return __builtin_bit_cast(float, u & 0xffff0000u); }
__device__ __forceinline__ f32x4 mfma16(u32x4 a, u32x4 b, f32x4 c) {
  return __builtin_amdgcn_mfma_f32_16x16x32_bf16(__builtin_bit_cast(bf16x8, a), __builtin_bit_cast(bf16x8, b), c, 0, 0, 0);
}
template <int CTRL> __device__ __forceinline__ float dpp(float x) {
  return __builtin_bit_cast(float, __builtin_amdgcn_mov_dpp(__builtin_bit_cast(int, x), CTRL, 0xf, 0xf, true));
}
__device__ __forceinline__ float xrow_max(float x) {
  auto s = __builtin_amdgcn_permlane16_swap(__float_as_uint(x), __float_as_uint(x), false, false);
  x = fmaxf(__uint_as_float(s[0]), __uint_as_float(s[1]));
  auto t = __builtin_amdgcn_permlane32_swap(__float_as_uint(x), __float_as_uint(x), false, false);
  return fmaxf(__uint_as_float(t[0]), __uint_as_float(t[1]));
}
__device__ __forceinline__ float xrow_sum(float x) {
  auto s = __builtin_amdgcn_permlane16_swap(__float_as_uint(x), __float_as_uint(x), false, false);
  x = __uint_as_float(s[0]) + __uint_as_float(s[1]);
  auto t = __builtin_amdgcn_permlane32_swap(__float_as_uint(x), __float_as_uint(x), false, false);
  return __uint_as_float(t[0]) + __uint_as_float(t[1]);
}
__device__ __forceinline__ rsrc_t mkrsrc(const void* p, unsigned bytes) { return __builtin_amdgcn_make_buffer_rsrc(const_cast<void*>(p), 0, bytes, 0x00020000); }
#define PLO 0x05040100u
#define PHI 0x07060302u
#define CFENCE() asm volatile("" ::: "memory")
#define SB() __builtin_amdgcn_sched_barrier(0)
#define AUX_NT 2
#define AUX_SC1 16

constexpr unsigned QS = 16384 + 512, OS = 8192 + 256, LS = 4096 + 256;
constexpr unsigned WS_EPOCH = 0;
constexpr unsigned WS_QG = 4096;
constexpr unsigned WS_OG = WS_QG + 64 * QS;
constexpr unsigned WS_LG = WS_OG + 64 * OS;
constexpr unsigned WS_NG = WS_LG + 32 * LS;
constexpr unsigned WS_END = WS_NG + 256 * 128;
constexpr unsigned WS_TS = 8 << 20;
constexpr int MREC = (4 + 128) * 4;
constexpr int L_M = 0;
constexpr int L_QT = L_M + NW * 4 * MREC;
constexpr int L_Q = L_QT + 8192;
constexpr int L_F = L_Q + 1024;
constexpr int L_R = L_F + 32;
constexpr int L_W = L_R + 2048;
constexpr int L_END = L_W + 16384;
enum { F_QP = 0, F_Q = 1, F_MERGE = 2, F_TAIL = 3, F_W = 4, F_OUT = 5, F_PM = 6, F_X = 7 };

__global__ void __launch_bounds__(NTHR, 2) fused_gqa(const unsigned char* __restrict__ x, const unsigned char* __restrict__ kc, const unsigned char* __restrict__ vc,
                                                      const unsigned char* __restrict__ wq, const unsigned char* __restrict__ wo, unsigned char* __restrict__ out, unsigned char* ws) {
  __shared__ __attribute__((aligned(16))) unsigned char lds_raw[L_END];
  LAS unsigned char* lds = (LAS unsigned char*)lds_raw;
  volatile LAS unsigned* flg = (volatile LAS unsigned*)(lds + L_F);
  unsigned long long tsv[8] = {0, 0, 0, 0, 0, 0, 0, 0};
#define TS(i) do { if ((i) == 0 || (i) == DIAGI) tsv[i] = __builtin_amdgcn_s_memrealtime(); } while (0)
  TS(0);
  asm volatile("" : "+s"(x), "+s"(kc), "+s"(vc), "+s"(wq), "+s"(wo), "+s"(out), "+s"(ws));
  const int blk = blockIdx.x, j = blk & 31, b = blk >> 5, kvh = j & 7, s = j >> 3, H = kvh * 4 + s;
  const int tid = threadIdx.x, lane = tid & 63, wave = __builtin_amdgcn_readfirstlane(tid >> 6), g = lane >> 4, c = lane & 15, w4 = wave & 3;
#define LINC(f) __builtin_amdgcn_atomic_inc32((LAS unsigned*)(lds + L_F + 4 * (f)), 0xffffffffu, __ATOMIC_RELAXED, "workgroup")
#define BUMP(f) do { CFENCE(); if (lane == 0) LINC(f); } while (0)
#define WAITF(f, n, slp) do { _Pragma("unroll 1") for (unsigned sp_ = 0; sp_ < (SPIN_CAP << 6); ++sp_) { if (flg[f] >= (unsigned)(n)) break; __builtin_amdgcn_s_sleep(slp); } CFENCE(); } while (0)
  const size_t kvoff = (size_t)(b * 8 + kvh) * SK * 256;
  const rsrc_t rk = mkrsrc(kc + kvoff, SK * 256), rv = mkrsrc(vc + kvoff, SK * 256);
  const rsrc_t rws = mkrsrc(ws, WS_END);
  unsigned kvo = (4 * (c & 3) + (c >> 2)) * 256 + g * 16, vvo = lane * 16;
  u32x4 kb[4][4], vb[4][4];
#define ISSUE_KV(st, so) do { asm volatile("" : "+v"(kvo), "+v"(vvo)); \
    _Pragma("unroll") for (int t_ = 0; t_ < 4; ++t_) { kb[st][t_] = __builtin_amdgcn_raw_buffer_load_b128(rk, kvo + t_ * 64, (so), AUX_NT); SB(); } \
    _Pragma("unroll") for (int t_ = 0; t_ < 4; ++t_) { vb[st][t_] = __builtin_amdgcn_raw_buffer_load_b128(rv, vvo + t_ * 1024, (so), AUX_NT); SB(); } \
    } while (0)
  const int w0 = (s * 4 + w4) * 4096 + (wave >> 2) * 4 * 65536;
  const unsigned ep = __builtin_amdgcn_raw_buffer_load_b32(rws, 0, WS_EPOCH, AUX_SC1);
  SB();
  unsigned tag;
  if (wave >= 4) {
    __builtin_amdgcn_s_setprio(3);
    const rsrc_t rx = mkrsrc(x, 8192), rq = mkrsrc(wq + (size_t)(s * 128 + w4 * 32) * 8192 + (kvh * 8 + b) * 128, WQSZ);
    const u32x4 xf = __builtin_amdgcn_raw_buffer_load_b128(rx, (c & 7) * 1024 + g * 16, s * 256 + w4 * 64, 0);
    u32x2 wr[8];
#pragma unroll
    for (int i = 0; i < 8; ++i) wr[i] = __builtin_amdgcn_raw_buffer_load_b64(rq, g * 65536 + c * 8, i * 8192, AUX_NT);
    const unsigned pw = __builtin_amdgcn_raw_buffer_load_b32(rws, lane < 4 ? WS_OG + (kvh * 8 + w4 * 2 + (lane >> 1)) * OS + (b * 4 + s) * 256 + (lane & 1) * 128
                                                                   : lane == 4 ? WS_LG + (kvh * 4 + w4) * LS + (b * 4 + s) * 128 : (QBF && lane < 9) ? WS_QG + (kvh * 8 + b) * QS + 8192 + ((s * 4 + w4) * 4 + lane - 5) * 128 : (NOTICE && lane == 9 && w4 == 0) ? WS_NG + ((kvh * 8 + b) * 4 + s) * 128 : OOB, 0, AUX_SC1);
    SB();
    if (tid < 256 + 8) flg[tid - 256] = 0u;
    asm volatile("s_waitcnt lgkmcnt(0)" ::: "memory"); __builtin_amdgcn_s_barrier(); CFENCE();
    SB();
    tag = __builtin_amdgcn_readfirstlane(ep) + 0x9E3779B9u;
#pragma unroll
    for (int w = 0; w < 2; ++w) {
      u32x4 a0, a1;
#pragma unroll
      for (int a = 0; a < 4; ++a) { a0[a] = __builtin_amdgcn_perm(wr[2 * a + 1][w], wr[2 * a][w], PLO); a1[a] = __builtin_amdgcn_perm(wr[2 * a + 1][w], wr[2 * a][w], PHI); }
      const f32x4 d0 = mfma16(a0, xf, f32x4{0.f, 0.f, 0.f, 0.f}), d1 = mfma16(a1, xf, f32x4{0.f, 0.f, 0.f, 0.f});
      if (c < 8) {
        *(LAS f32x4*)(lds + L_QT + w4 * 2048 + c * 256 + g * 64 + (2 * w) * 16) = d0;
        *(LAS f32x4*)(lds + L_QT + w4 * 2048 + c * 256 + g * 64 + (2 * w + 1) * 16) = d1;
      }
    }
    BUMP(F_QP);
    WAITF(F_QP, 4, 0);
    {
      const int p = lane & 31, ro = (2 * w4 + (lane >> 5)) * 256 + (p >> 3) * 64 + ((p >> 2) & 1) * 32 + (p & 3) * 4;
      float q0[4], q1[4];
#pragma unroll
      for (int w = 0; w < 4; ++w) { q0[w] = *(LAS float*)(lds + L_QT + w * 2048 + ro); q1[w] = *(LAS float*)(lds + L_QT + w * 2048 + ro + 16); }
      const float u0 = (q0[0] + q0[1]) + (q0[2] + q0[3]), u1 = (q1[0] + q1[1]) + (q1[2] + q1[3]);
      __builtin_amdgcn_raw_buffer_store_b64(u32x2{cvtpk(u0, u1), tag}, rws, (lane >> 5) * QS + p * 8, WS_QG + (kvh * 8 + 2 * w4) * QS + (b >> 1) * 2048 + s * 512 + (b & 1) * 256, AUX_SC1);
    }
    asm volatile("" :: "v"(pw));
    SB();
    __builtin_amdgcn_s_setprio(0);
    TS(1);
    ISSUE_KV(0, w0); ISSUE_KV(1, w0 + 65536); ISSUE_KV(2, w0 + 2 * 65536); ISSUE_KV(3, w0 + 3 * 65536);
  } else {
    ISSUE_KV(0, w0);
    __builtin_amdgcn_s_barrier(); CFENCE();
    SB();
    ISSUE_KV(1, w0 + 65536); ISSUE_KV(2, w0 + 2 * 65536); ISSUE_KV(3, w0 + 3 * 65536);
    tag = __builtin_amdgcn_readfirstlane(ep) + 0x9E3779B9u;
    TS(1);
    u32x4 qg[2];
#pragma unroll 1
    for (unsigned spin = 0; spin < SPIN_CAP; ++spin) {
      CFENCE();
      qg[0] = __builtin_amdgcn_raw_buffer_load_b128(rws, lane * 16, WS_QG + (kvh * 8 + b) * QS + w4 * 2048, AUX_SC1);
      qg[1] = __builtin_amdgcn_raw_buffer_load_b128(rws, lane * 16 + 1024, WS_QG + (kvh * 8 + b) * QS + w4 * 2048, AUX_SC1);
      if (__all((qg[0][1] == tag) & (qg[0][3] == tag) & (qg[1][1] == tag) & (qg[1][3] == tag))) break;
    }
    {
      float a0 = bflo(qg[0][0]) + bflo(qg[1][0]), a1 = bfhi(qg[0][0]) + bfhi(qg[1][0]), b0 = bflo(qg[0][2]) + bflo(qg[1][2]), b1 = bfhi(qg[0][2]) + bfhi(qg[1][2]);
      auto t0 = __builtin_amdgcn_permlane32_swap(__float_as_uint(a0), __float_as_uint(b0), false, false);
      auto t1 = __builtin_amdgcn_permlane32_swap(__float_as_uint(a1), __float_as_uint(b1), false, false);
      const int p = 2 * c + (lane >> 5);
      *(LAS unsigned*)(lds + L_Q + w4 * 256 + (g & 1) * 128 + (p >> 3) * 32 + (p & 3) * 8 + ((p >> 2) & 1) * 4) =
          cvtpk(__uint_as_float(t0[0]) + __uint_as_float(t0[1]), __uint_as_float(t1[0]) + __uint_as_float(t1[1]));
    }
    BUMP(F_Q);
  }
  unsigned v_rec = L_M + g * MREC, v_recd = v_rec + 16 + c * 32, v_dep = L_M + (wave * 4 + c) * MREC + 16 + g * 128;
  unsigned v_ost = (lane >> 3) * OS + (lane & 7) * 16, v_lst = g * LS + c * 16;
  unsigned v_wld = (32 * (g & 1) + 16 * (g >> 1)) * 1024 + c * 8;
  unsigned v_old = (c >> 2) * 256 + g * 64 + (c & 3) * 16, v_lld = g * 128 + (c & 3) * 16;
  unsigned v_yst = (g + (c >> 3)) * 512 + (c & 7) * 16, v_l8 = lane * 8, v_l16 = lane * 16;
#define RFL(x) (unsigned)__builtin_amdgcn_readfirstlane((int)(x))
  unsigned s_ost = RFL(WS_OG + kvh * 8 * OS + (b * 4 + s) * 256), s_lst = RFL(WS_LG + kvh * 4 * LS + (b * 4 + s) * 128);
  unsigned s_wld = RFL((64 * (w4 >> 1) + 4 * (w4 & 1)) * 1024), s_old = RFL(WS_OG + (H * 2) * OS + wave * 1024), s_lld = RFL(WS_LG + H * LS + wave * 512);
  unsigned s_yst = RFL(WS_QG + (kvh * 8 + wave) * QS + b * 2048 + s * 128), s_yld = RFL(WS_QG + (wave * 8 + b) * QS + j * 512);
  unsigned s_out = RFL(b * 1024 + (j >> 2) * 128 + (j & 1) * 64 + ((j >> 1) & 1) * 4), s_nst = RFL(WS_NG + ((kvh * 8 + b) * 4 + s) * 128), s_nld = RFL(WS_NG + (kvh * 8 + wave) * 512);
  const unsigned char* wtile = wo + (size_t)H * 131072 + b * 128;
  tag = __builtin_amdgcn_readfirstlane(tag);
#define PIN_TAIL() do { asm volatile("" : "+v"(v_rec), "+v"(v_recd), "+v"(v_dep), "+v"(v_ost), "+v"(v_lst), "+v"(v_wld), "+v"(v_old), "+v"(v_lld), "+v"(v_yst), "+v"(v_l8), "+v"(v_l16)); \
    asm volatile("" : "+s"(s_ost), "+s"(s_lst), "+s"(s_wld), "+s"(s_old), "+s"(s_lld), "+s"(s_yst), "+s"(s_yld), "+s"(s_out), "+s"(s_nst), "+s"(s_nld), "+s"(wtile)); } while (0)
  PIN_TAIL();
  WAITF(F_Q, 4, 1);
  TS(2);
  u32x4 qf[4];
#pragma unroll
  for (int t = 0; t < 4; ++t) qf[t] = *(LAS u32x4*)(lds + L_Q + (c & 3) * 256 + (4 * t + g) * 16);

  float m = -INFINITY, l = 0.f;
  f32x4 acc[8];
#pragma unroll
  for (int i = 0; i < 8; ++i) acc[i] = f32x4{0.f, 0.f, 0.f, 0.f};
  auto tile = [&](auto stc) __attribute__((always_inline)) {
    constexpr int st = decltype(stc)::value;
    f32x4 s0 = {0.f, 0.f, 0.f, 0.f}, s1 = {0.f, 0.f, 0.f, 0.f};
#pragma unroll
    for (int t = 0; t < 4; ++t) { s0 = mfma16(kb[st][t], qf[t], s0); s1 = mfma16(kb[st + 1][t], qf[t], s1); }
    float pm = fmaxf(fmaxf(fmaxf(s0[0], s0[1]), fmaxf(s0[2], s0[3])), fmaxf(fmaxf(s1[0], s1[1]), fmaxf(s1[2], s1[3])));
    pm = xrow_max(pm);
    if (__any(pm > m)) {
      const float mn = fmaxf(m, pm);
      const float alpha = __builtin_amdgcn_exp2f((m - mn) * C2);
      m = mn; l *= alpha;
#pragma unroll
      for (int i = 0; i < 8; ++i) acc[i] *= alpha;
    }
    const float mc = -m * C2;
    float p[8];
#pragma unroll
    for (int r = 0; r < 4; ++r) { p[r] = __builtin_amdgcn_exp2f(fmaf(s0[r], C2, mc)); p[4 + r] = __builtin_amdgcn_exp2f(fmaf(s1[r], C2, mc)); }
    l += ((p[0] + p[1]) + (p[2] + p[3])) + ((p[4] + p[5]) + (p[6] + p[7]));
    const u32x4 pf = {cvtpk(p[0], p[1]), cvtpk(p[2], p[3]), cvtpk(p[4], p[5]), cvtpk(p[6], p[7])};
#pragma unroll
    for (int w = 0; w < 4; ++w) {
      u32x4 a0, a1;
      a0[0] = __builtin_amdgcn_perm(vb[st][1][w], vb[st][0][w], PLO);         a1[0] = __builtin_amdgcn_perm(vb[st][1][w], vb[st][0][w], PHI);
      a0[1] = __builtin_amdgcn_perm(vb[st][3][w], vb[st][2][w], PLO);         a1[1] = __builtin_amdgcn_perm(vb[st][3][w], vb[st][2][w], PHI);
      a0[2] = __builtin_amdgcn_perm(vb[st + 1][1][w], vb[st + 1][0][w], PLO); a1[2] = __builtin_amdgcn_perm(vb[st + 1][1][w], vb[st + 1][0][w], PHI);
      a0[3] = __builtin_amdgcn_perm(vb[st + 1][3][w], vb[st + 1][2][w], PLO); a1[3] = __builtin_amdgcn_perm(vb[st + 1][3][w], vb[st + 1][2][w], PHI);
      acc[2 * w] = mfma16(a0, pf, acc[2 * w]);
      acc[2 * w + 1] = mfma16(a1, pf, acc[2 * w + 1]);
    }
    SB();
  };
  auto rescale = [&](float pm) __attribute__((always_inline)) {
    if (__any(pm > m)) {
      const float mn = fmaxf(m, pm);
      const float alpha = __builtin_amdgcn_exp2f((m - mn) * C2);
      m = mn; l *= alpha;
#pragma unroll
      for (int i = 0; i < 8; ++i) acc[i] *= alpha;
    }
  };
  auto tile2 = [&](auto stc) __attribute__((always_inline)) {
    constexpr int st = decltype(stc)::value;
    const int gam = c >> 2;
    float sc[2];
#pragma unroll
    for (int T = 0; T < 2; ++T) {
      f32x4 d[4];
#pragma unroll
      for (int i = 0; i < 4; ++i) d[i] = mfma16(kb[st + T][i], qf[i], f32x4{0.f, 0.f, 0.f, 0.f});
      float X[4];
#pragma unroll
      for (int u = 0; u < 4; ++u) X[u] = (gam & 2) ? ((gam & 1) ? d[(3 - u) & 3][u] : d[(2 - u) & 3][u]) : ((gam & 1) ? d[(1 - u) & 3][u] : d[(0 - u) & 3][u]);
      sc[T] = (X[0] + dpp<0x12C>(X[1])) + (dpp<0x128>(X[2]) + dpp<0x124>(X[3]));
    }
    float pm = fmaxf(sc[0], sc[1]);
    pm = fmaxf(pm, dpp<0x124>(pm)); pm = fmaxf(pm, dpp<0x128>(pm));
    pm = xrow_max(pm);
    rescale(pm);
    const float mc = -m * C2;
    const float p0 = __builtin_amdgcn_exp2f(fmaf(sc[0], C2, mc)), p1 = __builtin_amdgcn_exp2f(fmaf(sc[1], C2, mc));
    float ps = p0 + p1; ps += dpp<0x128>(ps); ps += dpp<0x124>(ps);
    l += ps;
    const float pk = __uint_as_float(cvtpk(p0, p1));
    const u32x4 pf = {__float_as_uint(pk), __float_as_uint(dpp<0x12C>(pk)), __float_as_uint(dpp<0x128>(pk)), __float_as_uint(dpp<0x124>(pk))};
#pragma unroll
    for (int w = 0; w < 4; ++w) {
      u32x4 a0, a1;
#pragma unroll
      for (int a = 0; a < 4; ++a) { a0[a] = __builtin_amdgcn_perm(vb[st + 1][a][w], vb[st][a][w], PLO); a1[a] = __builtin_amdgcn_perm(vb[st + 1][a][w], vb[st][a][w], PHI); }
      acc[2 * w] = mfma16(a0, pf, acc[2 * w]);
      acc[2 * w + 1] = mfma16(a1, pf, acc[2 * w + 1]);
    }
    SB();
  };
#define ISSUE_KV2(st, so) do { \
    _Pragma("unroll") for (int t_ = 0; t_ < 4; ++t_) { kb[st][t_] = __builtin_amdgcn_raw_buffer_load_b128(rk, ko[t_], (so), AUX_NT); SB(); } \
    _Pragma("unroll") for (int t_ = 0; t_ < 4; ++t_) { vb[st][t_] = __builtin_amdgcn_raw_buffer_load_b128(rv, vvo + t_ * 1024, (so), AUX_NT); SB(); } \
    } while (0)
  if (wave >= 4) {
#pragma unroll 1
    for (int it = 0; it < 6; ++it) {
      const int so0 = w0 + (it + 1) * 4 * 65536;
      unsigned ko[4];
      ko[0] = it == 5 ? (c >> 2) * 256 + g * 64 + (c & 3) * 16 : kvo;
#pragma unroll
      for (int t = 1; t < 4; ++t) ko[t] = ko[0] + t * (it == 5 ? 1024 : 64);
      tile(std::integral_constant<int, 0>{});
      asm volatile("" : "+v"(vvo), "+v"(ko[0]), "+v"(ko[1]), "+v"(ko[2]), "+v"(ko[3])); SB();
      ISSUE_KV2(0, so0); ISSUE_KV2(1, so0 + 65536);
      tile(std::integral_constant<int, 2>{});
      asm volatile("" : "+v"(vvo)); SB();
      ISSUE_KV2(2, so0 + 2 * 65536); ISSUE_KV2(3, so0 + 3 * 65536);
    }
#pragma unroll
    for (int i = 0; i < 4; ++i) qf[i] = *(LAS u32x4*)(lds + L_Q + (c & 3) * 256 + (4 * g + (((c >> 2) - i) & 3)) * 16);
    tile2(std::integral_constant<int, 0>{});
    tile2(std::integral_constant<int, 2>{});
  } else {
    tile(std::integral_constant<int, 0>{});
    tile(std::integral_constant<int, 2>{});
  }
  l = xrow_sum(l);
  if (c < 4) {
#pragma unroll
    for (int i = 0; i < 8; ++i) *(LAS f32x4*)(lds + v_dep + i * 16) = acc[i];
    if (g == 0) *(LAS f32x2*)(lds + v_dep - 16) = f32x2{m * C2, l};
  }
  CFENCE();
  constexpr int W0 = 3;
  if (wave < 4) {
    unsigned a4 = 0;
    if (lane == 0) a4 = LINC(F_PM);
    if (__builtin_amdgcn_readfirstlane(a4) == 3) {
      CFENCE();
      f32x2 ml[4]; f32x4 a0[4], a1[4];
#pragma unroll
      for (int w = 0; w < 4; ++w) { LAS unsigned char* rec = lds + L_M + (w * 4 + g) * MREC; ml[w] = *(LAS f32x2*)rec; a0[w] = *(LAS f32x4*)(rec + 16 + c * 32); a1[w] = *(LAS f32x4*)(rec + 32 + c * 32); }
      const float M = fmaxf(fmaxf(ml[0][0], ml[1][0]), fmaxf(ml[2][0], ml[3][0]));
      float L = 0.f; f32x4 o0 = {0.f, 0.f, 0.f, 0.f}, o1 = {0.f, 0.f, 0.f, 0.f};
#pragma unroll
      for (int w = 0; w < 4; ++w) { const float wt = __builtin_amdgcn_exp2f(ml[w][0] - M); L = fmaf(wt, ml[w][1], L); o0 += wt * a0[w]; o1 += wt * a1[w]; }
      LAS unsigned char* rec = lds + L_M + (3 * 4 + g) * MREC;
      CFENCE();
      *(LAS f32x4*)(rec + 16 + c * 32) = o0; *(LAS f32x4*)(rec + 32 + c * 32) = o1;
      if (c == 0) *(LAS f32x2*)rec = f32x2{M, L};
      CFENCE();
    }
  }
  unsigned arr = 0;
  if (lane == 0) arr = LINC(F_MERGE);
  const unsigned arru = __builtin_amdgcn_readfirstlane(arr);
  CFENCE();
  TS(3);
  constexpr unsigned live = ~0u;
  {
    const rsrc_t rwx = mkrsrc(ws, WS_END & live);
    const unsigned tagx = tag & live, tagy = (tag + 1u) & live;
    const int fW = live ? F_W : F_X, fO = live ? F_OUT : F_X;
    if ((arru | (~live & 7u)) == NW - 1) {
      f32x2 ml[NW]; f32x4 a0[NW], a1[NW];
#pragma unroll
      for (int w = W0; w < NW; ++w) ml[w] = *(LAS f32x2*)(lds + v_rec + w * 4 * MREC);
#pragma unroll
      for (int w = W0; w < NW; ++w) { a0[w] = *(LAS f32x4*)(lds + v_recd + w * 4 * MREC); a1[w] = *(LAS f32x4*)(lds + v_recd + w * 4 * MREC + 16); }
      float M = ml[W0][0];
#pragma unroll
      for (int w = W0 + 1; w < NW; ++w) M = fmaxf(M, ml[w][0]);
      float L = 0.f; f32x4 o0 = {0.f, 0.f, 0.f, 0.f}, o1 = {0.f, 0.f, 0.f, 0.f};
#pragma unroll
      for (int w = W0; w < NW; ++w) { const float wt = __builtin_amdgcn_exp2f(ml[w][0] - M); L = fmaf(wt, ml[w][1], L); o0 += wt * a0[w]; o1 += wt * a1[w]; }
      const float inv = __builtin_amdgcn_rcpf(L);
      o0 *= inv; o1 *= inv;
      const unsigned lse = __float_as_uint(M + __builtin_amdgcn_logf(L));
      u32x4 d0 = {cvtpk(o0[0], o1[0]), tag, cvtpk(o0[1], o1[1]), tag}, d1 = {cvtpk(o0[2], o1[2]), tag, cvtpk(o0[3], o1[3]), tag}, d2 = {lse, tag, lse, tag};
      asm volatile("" : "+v"(d0), "+v"(d1), "+v"(d2));
      __builtin_amdgcn_raw_buffer_store_b128(d0, rwx, v_ost, s_ost, AUX_SC1);
      __builtin_amdgcn_raw_buffer_store_b128(d1, rwx, v_ost + 128, s_ost, AUX_SC1);
      if (c < 8) __builtin_amdgcn_raw_buffer_store_b128(d2, rwx, v_lst, s_lst, AUX_SC1);
      TS(4);
    }
    if (wave < 4) {
      WAITF(F_MERGE, 5u & live, LSL);
      WAITF(F_MERGE, (LATEW - 4) & live, WSL);
      const rsrc_t rw = mkrsrc(wtile, 131072 & live);
      u32x2 wr[8];
#pragma unroll
      for (int i = 0; i < 8; ++i) wr[i] = __builtin_amdgcn_raw_buffer_load_b64(rw, v_wld + (2 * (i >> 2) + (i & 1)) * 1024, s_wld + 8 * ((i >> 1) & 1) * 1024, WAUX);
#pragma unroll
      for (int w = 0; w < 2; ++w) {
        u32x4 f0, f1;
#pragma unroll
        for (int a = 0; a < 4; ++a) { f0[a] = __builtin_amdgcn_perm(wr[2 * a + 1][w], wr[2 * a][w], PLO); f1[a] = __builtin_amdgcn_perm(wr[2 * a + 1][w], wr[2 * a][w], PHI); }
        *(LAS u32x4*)(lds + L_W + (w4 * 4 + 2 * w) * 1024 + v_l16) = f0; *(LAS u32x4*)(lds + L_W + (w4 * 4 + 2 * w + 1) * 1024 + v_l16) = f1;
      }
      BUMP(fW);
    }
    WAITF(F_W, 4u & live, WSL);
    u32x4 wf[16], og0, og1, lg;
#pragma unroll
    for (int i = 0; i < 16; ++i) wf[i] = *(LAS u32x4*)(lds + L_W + i * 1024 + v_l16);
    {
      bool no = true, nl = true;
#pragma unroll 1
      for (unsigned spin = 0; spin < SPIN_CAP; ++spin) {
        CFENCE();
        if (no) {
          og0 = __builtin_amdgcn_raw_buffer_load_b128(rwx, v_old, s_old, AUX_SC1);
          og1 = __builtin_amdgcn_raw_buffer_load_b128(rwx, v_old, s_old + OS, AUX_SC1);
        }
        if (nl) lg = __builtin_amdgcn_raw_buffer_load_b128(rwx, v_lld, s_lld, AUX_SC1);
        no = !((og0[1] == tagx) & (og0[3] == tagx) & (og1[1] == tagx) & (og1[3] == tagx));
        nl = !((lg[1] == tagx) & (lg[3] == tagx));
        if (!__any(no | nl)) break;
        __builtin_amdgcn_s_sleep(PSLEEP);
      }
    }
    TS(5);
    {
      const int u = c & 3;
      const u32x4 a0 = {u == 0 ? og0[0] : 0u, u == 0 ? og0[2] : 0u, u == 1 ? og0[0] : 0u, u == 1 ? og0[2] : 0u};
      const u32x4 a1 = {u == 2 ? og0[0] : 0u, u == 2 ? og0[2] : 0u, u == 3 ? og0[0] : 0u, u == 3 ? og0[2] : 0u};
      const u32x4 a2 = {u == 0 ? og1[0] : 0u, u == 0 ? og1[2] : 0u, u == 1 ? og1[0] : 0u, u == 1 ? og1[2] : 0u};
      const u32x4 a3 = {u == 2 ? og1[0] : 0u, u == 2 ? og1[2] : 0u, u == 3 ? og1[0] : 0u, u == 3 ? og1[2] : 0u};
      const float lse = __uint_as_float(lg[0]);
      const float e = __builtin_amdgcn_exp2f(lse - xrow_max(lse));
      const float wt = e * __builtin_amdgcn_rcpf(xrow_sum(e));
      f32x4 d[4];
#pragma unroll
      for (int i = 0; i < 4; ++i) d[i] = mfma16(a0, wf[i], f32x4{0.f, 0.f, 0.f, 0.f});
#pragma unroll
      for (int i = 0; i < 4; ++i) d[i] = mfma16(a1, wf[4 + i], d[i]);
#pragma unroll
      for (int i = 0; i < 4; ++i) d[i] = mfma16(a2, wf[8 + i], d[i]);
#pragma unroll
      for (int i = 0; i < 4; ++i) d[i] = mfma16(a3, wf[12 + i], d[i]);
      float y[4];
#pragma unroll
      for (int i = 0; i < 4; ++i) y[i] = wt * ((d[i][0] + d[i][1]) + (d[i][2] + d[i][3]));
#pragma unroll
      for (int i = 0; i < 2; ++i) { auto t = __builtin_amdgcn_permlane32_swap(__float_as_uint(y[i]), __float_as_uint(y[i + 2]), false, false); y[i] = __uint_as_float(t[0]) + __uint_as_float(t[1]); }
#pragma unroll
      for (int i = 0; i < 2; ++i) { auto t = __builtin_amdgcn_permlane16_swap(__float_as_uint(y[i]), __float_as_uint(y[i]), false, false); y[i] = __uint_as_float(t[0]) + __uint_as_float(t[1]); }
      if ((g & 1) == 0) __builtin_amdgcn_raw_buffer_store_b128(u32x4{__float_as_uint(y[0]), tagy, __float_as_uint(y[1]), tagy}, rwx, v_yst, s_yst, AUX_SC1);
    }
    TS(6);
    u32x4 yg;
    {
      bool ny = lane < 32;
#pragma unroll 1
      for (unsigned spin = 0; spin < SPIN_CAP; ++spin) {
        CFENCE();
        if (ny) { yg = __builtin_amdgcn_raw_buffer_load_b128(rwx, v_l16, s_yld, AUX_SC1); ny = !((yg[1] == tagy) & (yg[3] == tagy)); }
        if (!__any(ny)) break;
        __builtin_amdgcn_s_sleep(PSLEEP2);
      }
    }
    if (lane < 32) *(LAS u32x2*)(lds + L_R + wave * 256 + v_l8) = u32x2{yg[0], yg[2]};
    CFENCE();
    unsigned arr2 = 0;
    if (lane == 0) arr2 = LINC(fO);
    if ((__builtin_amdgcn_readfirstlane(arr2) | (~live & 7u)) == NW - 1) {
      CFENCE();
      f32x2 p[4];
#pragma unroll
      for (int w = 0; w < 4; ++w) p[w] = *(LAS f32x2*)(lds + L_R + w * 512 + v_l8);
      const f32x2 ps = (p[0] + p[1]) + (p[2] + p[3]);
      float r0 = ps[0], r1 = ps[1];
      r0 += dpp<0x128>(r0); r1 += dpp<0x128>(r1);
      r0 = xrow_sum(r0); r1 = xrow_sum(r1);
      if (lane < 8) __builtin_amdgcn_raw_buffer_store_b32(cvtpk(r0, r1), mkrsrc(out, 8192 & live), v_l8, s_out, 0);
      if (lane == 0 && blk == 0) __builtin_amdgcn_raw_buffer_store_b32(tag, rwx, 0, WS_EPOCH, AUX_SC1);
      TS(7);
    }
  }
  if ((DIAGB < 0 || blk == DIAGB) && (DIAGW < 0 || wave == DIAGW) && tsv[DIAGI]) { const unsigned long long due = tsv[0] + 10000 + 2 * (tsv[DIAGI] - tsv[0]); while (__builtin_amdgcn_s_memrealtime() < due) __builtin_amdgcn_s_sleep(4); }
}

extern "C" void kernel_launch(void* const* d_in, const int* in_sizes, int n_in, void* d_out, int out_size, void* d_ws, size_t ws_size, hipStream_t stream) {
  fused_gqa<<<256, NTHR, 0, stream>>>((const unsigned char*)d_in[0], (const unsigned char*)d_in[1], (const unsigned char*)d_in[2], (const unsigned char*)d_in[3], (const unsigned char*)d_in[4],
                                      (unsigned char*)d_out, (unsigned char*)d_ws);
}
```

```cpp
#include <hip/hip_runtime.h>
#include <hip/hip_bf16.h>
#include <cstdint>
#include <type_traits>

#define WTRIG 3
#define PSLEEP 4
#define PSLEEP2 2
#define KSP 4
#define VSP 2
#define HSP 4
#define WQSZ (32 * 8192)
#define PWQ 0
#define PWOFF 0
#define NTRIG 0
#define NOTAT 1
#define NSL 4
#define LX 1
#define LXS 127
#define PWL 1
#define GATE 1
#define GDLY 0
#define TOPBAR 0
#define FARU 16
constexpr int SK = 8192, NW = 8, NTHR = NW * 64;
constexpr float C2 = 0.088388347648318440f * 1.4426950408889634f;
constexpr unsigned SPIN_CAP = 1u << 16;
constexpr unsigned OOB = 0x40000000u;

using bf16x8 = __attribute__((ext_vector_type(8))) __bf16;
using bf16x2 = __attribute__((ext_vector_type(2))) __bf16;
using u32x4  = __attribute__((ext_vector_type(4))) unsigned;
using u32x2  = __attribute__((ext_vector_type(2))) unsigned;
using f32x4  = __attribute__((ext_vector_type(4))) float;
using f32x2  = __attribute__((ext_vector_type(2))) float;
using rsrc_t = __amdgpu_buffer_rsrc_t;
#define LAS __attribute__((address_space(3)))

__device__ __forceinline__ unsigned cvtpk(float lo, float hi) { return __builtin_bit_cast(unsigned, __builtin_convertvector(f32x2{lo, hi}, bf16x2)); }
__device__ __forceinline__ float bflo(unsigned u) { return __builtin_bit_cast(float, u << 16); }
__device__ __forceinline__ float bfhi(unsigned u) { return __builtin_bit_cast(float, u & 0xffff0000u); }
__device__ __forceinline__ f32x4 mfma16(u32x4 a, u32x4 b, f32x4 c) {
  return __builtin_amdgcn_mfma_f32_16x16x32_bf16(__builtin_bit_cast(bf16x8, a), __builtin_bit_cast(bf16x8, b), c, 0, 0, 0);
}
template <int CTRL> __device__ __forceinline__ float dpp(float x) {
  return __builtin_bit_cast(float, __builtin_amdgcn_mov_dpp(__builtin_bit_cast(int, x), CTRL, 0xf, 0xf, true));
}
__device__ __forceinline__ float xrow_max(float x) {
  auto s = __builtin_amdgcn_permlane16_swap(__float_as_uint(x), __float_as_uint(x), false, false);
  x = fmaxf(__uint_as_float(s[0]), __uint_as_float(s[1]));
  auto t = __builtin_amdgcn_permlane32_swap(__float_as_uint(x), __float_as_uint(x), false, false);
  return fmaxf(__uint_as_float(t[0]), __uint_as_float(t[1]));
}
__device__ __forceinline__ float xrow_sum(float x) {
  auto s = __builtin_amdgcn_permlane16_swap(__float_as_uint(x), __float_as_uint(x), false, false);
  x = __uint_as_float(s[0]) + __uint_as_float(s[1]);
  auto t = __builtin_amdgcn_permlane32_swap(__float_as_uint(x), __float_as_uint(x), false, false);
  return __uint_as_float(t[0]) + __uint_as_float(t[1]);
}
__device__ __forceinline__ rsrc_t mkrsrc(const void* p, unsigned bytes) { return __builtin_amdgcn_make_buffer_rsrc(const_cast<void*>(p), 0, bytes, 0x00020000); }
#define PLO 0x05040100u
#define PHI 0x07060302u
#define CFENCE() asm volatile("" ::: "memory")
#define SB() __builtin_amdgcn_sched_barrier(0)
#define RFL(x) (unsigned)__builtin_amdgcn_readfirstlane((int)(x))
#define AUX_NT 2
#define AUX_SC1 16

constexpr unsigned QS = 16384 + 512, OS = 8192 + 256;
constexpr unsigned WS_EPOCH = 0;
constexpr unsigned WS_QG = 4096;
constexpr unsigned WS_OG = WS_QG + 64 * QS;
constexpr unsigned WS_LG = WS_OG + 64 * OS;
constexpr unsigned WS_NG = WS_LG + 64 * 2048;
constexpr unsigned WS_END = WS_NG + 256 * 128;
constexpr unsigned WS_TS = 8 << 20;
constexpr int MREC = 512 + 16;
constexpr int L_M = 0;
constexpr int L_QT = L_M + 16 * MREC;
constexpr int L_Q = L_QT + 8192;
constexpr int L_F = L_Q + 1024;
constexpr int L_HM = L_F + 64;
constexpr int L_ML = L_HM + 64;
constexpr int L_FA = L_ML + 128;
constexpr int L_R = L_FA + 64;
constexpr int L_S = L_R + 2048;
constexpr int L_P = L_S + 32768;
constexpr int L_W = L_P + 16384;
constexpr int L_END = L_W + 16384;
enum { F_QP = 0, F_Q, F_HM, F_L, F_F, F_MERGE, F_W, F_OUT, F_N };
constexpr int USTR = 65536;
constexpr int STG = FARU == 16 ? USTR : 2 * USTR, UB = FARU * USTR;

__global__ void __launch_bounds__(NTHR, 2) fused_gqa(const unsigned char* __restrict__ x, const unsigned char* __restrict__ kc, const unsigned char* __restrict__ vc,
                                                      const unsigned char* __restrict__ wq, const unsigned char* __restrict__ wo, unsigned char* __restrict__ out, unsigned char* ws) {
  __shared__ __attribute__((aligned(16))) unsigned char lds_raw[L_END];
  LAS unsigned char* lds = (LAS unsigned char*)lds_raw;
  volatile LAS unsigned* flg = (volatile LAS unsigned*)(lds + L_F);
#define TS(i)
  TS(0);
  asm volatile("" : "+s"(x), "+s"(kc), "+s"(vc), "+s"(wq), "+s"(wo), "+s"(out), "+s"(ws));
  const int blk = blockIdx.x, j = blk & 31, b = blk >> 5, kvh = j & 7, s = j >> 3, H = kvh * 4 + s;
  const int tid = threadIdx.x, lane = tid & 63, wave = __builtin_amdgcn_readfirstlane(tid >> 6), g = lane >> 4, c = lane & 15, w4 = wave & 3;
#define LINC(f) __builtin_amdgcn_atomic_inc32((LAS unsigned*)(lds + L_F + 4 * (f)), 0xffffffffu, __ATOMIC_RELAXED, "workgroup")
#define BUMP(f) do { CFENCE(); if (lane == 0) LINC(f); } while (0)
#define WAITF(f, n, slp) do { _Pragma("unroll 1") for (unsigned sp_ = 0; sp_ < (SPIN_CAP << 6); ++sp_) { if (flg[f] >= (unsigned)(n)) break; __builtin_amdgcn_s_sleep(slp); } CFENCE(); } while (0)
  const size_t kvoff = (size_t)(b * 8 + kvh) * SK * 256;
  const unsigned char* kp = kc + kvoff; const unsigned char* vp = vc + kvoff;
  const rsrc_t rk = mkrsrc(kp, SK * 256), rv = mkrsrc(vp, SK * 256);
  const rsrc_t rws = mkrsrc(ws, WS_END);
  unsigned k2o = (c >> 2) * 256 + g * 64 + (c & 3) * 16, vvo = lane * 16;
#define ISSUE(st, rs, vo, so, nsp) do { int sa_ = (so), sb_ = (so) + UB; asm volatile("" : "+v"(vo), "+s"(sa_), "+s"(sb_)); SB(); \
    _Pragma("unroll") for (int t_ = 0; t_ < 4; ++t_) { \
      if (t_ < (nsp)) { const u32x2 lo_ = __builtin_amdgcn_raw_buffer_load_b64(rs, vo + t_ * 1024, sa_, AUX_NT); SB(); const u32x2 hi_ = __builtin_amdgcn_raw_buffer_load_b64(rs, vo + t_ * 1024 + 8, sa_, AUX_NT); SB(); \
                        ba[st][t_] = u32x4{lo_[0], lo_[1], hi_[0], hi_[1]}; } \
      else { ba[st][t_] = __builtin_amdgcn_raw_buffer_load_b128(rs, vo + t_ * 1024, sa_, AUX_NT); SB(); } } \
    _Pragma("unroll") for (int t_ = 0; t_ < 4; ++t_) { bb[st][t_] = __builtin_amdgcn_raw_buffer_load_b128(rs, vo + t_ * 1024, sb_, AUX_NT); SB(); } \
    } while (0)
  const int u0 = (s * 4 + w4) * 4096, w0 = u0 + (wave >> 2) * 4 * STG;
  const unsigned ep = __builtin_amdgcn_raw_buffer_load_b32(rws, 0, WS_EPOCH, AUX_SC1);
  SB();
  unsigned v_recd, v_dep, v_ost, v_wld, v_yst, v_l8, s_ost, s_wld, s_old, s_yst, s_yld, s_out;
  const unsigned char* wtile;
  using I0 = std::integral_constant<int, 0>; using I1 = std::integral_constant<int, 1>; using I2 = std::integral_constant<int, 2>; using I3 = std::integral_constant<int, 3>;
  const int tau = c >> 2;
  unsigned tag;
  if (wave >= 4) {
    u32x4 ba[4][4], bb[4][4];
    __builtin_amdgcn_s_setprio(3);
    const rsrc_t rx = mkrsrc(x, 8192), rq = mkrsrc(wq + (size_t)(s * 128 + w4 * 32) * 8192 + (kvh * 8 + b) * 128, WQSZ);
    const u32x4 xf = __builtin_amdgcn_raw_buffer_load_b128(rx, (c & 7) * 1024 + g * 16, s * 256 + w4 * 64, 0);
    u32x2 wr[8];
#pragma unroll
    for (int i = 0; i < 8; ++i) wr[i] = __builtin_amdgcn_raw_buffer_load_b64(rq, g * 65536 + c * 8, i * 8192, AUX_NT);
    const unsigned pw = __builtin_amdgcn_raw_buffer_load_b32(rws, (LX == 1 && PWL && lane == 13 && w4 == 1) ? WS_LG + (b * 8 + kvh) * 512 + s * 128 : (LX == 0 && PWL && lane == 13) ? WS_LG + (b * 8 + kvh) * 2048 + w4 * 512 + s * 128 : lane < 4 ? WS_OG + (kvh * 8 + w4 * 2 + (lane >> 1)) * OS + (b * 4 + s) * 256 + (lane & 1) * 128
                                                                   : lane < 8 ? WS_QG + (kvh * 8 + b) * QS + 8192 + ((s * 4 + w4) * 4 + lane - 4) * 128 : (NTRIG && lane == 12 && w4 == 0) ? WS_NG + ((kvh * 8 + b) * 4 + s) * 128 : (PWQ && lane < 12) ? WS_QG + (kvh * 8 + b) * QS + ((s * 4 + w4) * 4 + lane - 8) * 128 : OOB, PWOFF, AUX_SC1);
    SB();
    if (tid < 256 + 16) flg[tid - 256] = 0u;
    asm volatile("s_waitcnt lgkmcnt(0)" ::: "memory"); __builtin_amdgcn_s_barrier(); CFENCE();
    SB();
    tag = __builtin_amdgcn_readfirstlane(ep) + 0x9E3779B9u;
#pragma unroll
    for (int w = 0; w < 2; ++w) {
      u32x4 a0, a1;
#pragma unroll
      for (int a = 0; a < 4; ++a) { a0[a] = __builtin_amdgcn_perm(wr[2 * a + 1][w], wr[2 * a][w], PLO); a1[a] = __builtin_amdgcn_perm(wr[2 * a + 1][w], wr[2 * a][w], PHI); }
      const f32x4 d0 = mfma16(a0, xf, f32x4{0.f, 0.f, 0.f, 0.f}), d1 = mfma16(a1, xf, f32x4{0.f, 0.f, 0.f, 0.f});
      if (c < 8) {
        *(LAS f32x4*)(lds + L_QT + w4 * 2048 + c * 256 + g * 64 + (2 * w) * 16) = d0;
        *(LAS f32x4*)(lds + L_QT + w4 * 2048 + c * 256 + g * 64 + (2 * w + 1) * 16) = d1;
      }
    }
    BUMP(F_QP);
    WAITF(F_QP, 4, 0);
    {
      const int p = lane & 31, ro = (2 * w4 + (lane >> 5)) * 256 + (p >> 3) * 64 + ((p >> 2) & 1) * 32 + (p & 3) * 4;
      float q0[4], q1[4];
#pragma unroll
      for (int w = 0; w < 4; ++w) { q0[w] = *(LAS float*)(lds + L_QT + w * 2048 + ro); q1[w] = *(LAS float*)(lds + L_QT + w * 2048 + ro + 16); }
      const float t0 = (q0[0] + q0[1]) + (q0[2] + q0[3]), t1 = (q1[0] + q1[1]) + (q1[2] + q1[3]);
      __builtin_amdgcn_raw_buffer_store_b64(u32x2{cvtpk(t0, t1), tag}, rws, (lane >> 5) * QS + p * 8, WS_QG + (kvh * 8 + 2 * w4) * QS + (b >> 1) * 2048 + s * 512 + (b & 1) * 256, AUX_SC1);
    }
    asm volatile("" :: "v"(pw));
    SB();
    __builtin_amdgcn_s_setprio(0);
    TS(1);
    ISSUE(0, rk, k2o, w0, KSP); ISSUE(1, rk, k2o, w0 + STG, KSP); ISSUE(2, rk, k2o, w0 + 2 * STG, KSP); ISSUE(3, rk, k2o, w0 + 3 * STG, KSP);
  v_recd = L_M + g * MREC + c * 32, v_dep = L_M + (w4 * 4 + c) * MREC + g * 128;
  v_ost = (lane >> 3) * OS + (lane & 7) * 16;
  v_wld = (32 * (g & 1) + 16 * (g >> 1)) * 1024 + c * 8;
  v_yst = (g + (c >> 3)) * 512 + (c & 7) * 16, v_l8 = lane * 8;
  s_ost = RFL(WS_OG + kvh * 8 * OS + (b * 4 + s) * 256), s_wld = RFL((64 * (w4 >> 1) + 4 * (w4 & 1)) * 1024), s_old = RFL(WS_OG + (H * 2) * OS + wave * 1024);
  s_yst = RFL(WS_QG + (kvh * 8 + wave) * QS + b * 2048 + s * 128), s_yld = RFL(WS_QG + (wave * 8 + b) * QS + j * 512);
  s_out = RFL(b * 1024 + (j >> 2) * 128 + (j & 1) * 64 + ((j >> 1) & 1) * 4);
  wtile = wo + (size_t)H * 131072 + b * 128;
  tag = RFL(tag);
  asm volatile("" : "+v"(v_recd), "+v"(v_dep), "+v"(v_ost), "+v"(v_wld), "+v"(v_yst), "+v"(v_l8));
  asm volatile("" : "+s"(s_ost), "+s"(s_wld), "+s"(s_old), "+s"(s_yst), "+s"(s_yld), "+s"(s_out), "+s"(wtile));
  WAITF(F_Q, 4, 1);
  TS(2);

  u32x4 qf[4];
#pragma unroll
  for (int t = 0; t < 4; ++t) qf[t] = *(LAS u32x4*)(lds + L_Q + (c & 3) * 256 + (4 * g + ((tau - t) & 3)) * 16);
  float mx = -INFINITY;
  unsigned sadr = L_S + w4 * 8192 + (wave >> 2) * 2048 + lane * 8;
  auto scoreK = [&](auto stc) __attribute__((always_inline)) {
    constexpr int st = decltype(stc)::value;
    f32x4 d[4], e[4];
#pragma unroll
    for (int i = 0; i < 4; ++i) d[i] = mfma16(ba[st][i], qf[i], f32x4{0.f, 0.f, 0.f, 0.f});
#pragma unroll
    for (int i = 0; i < 4; ++i) e[i] = mfma16(bb[st][i], qf[i], f32x4{0.f, 0.f, 0.f, 0.f});
    float X[4], Y[4];
#pragma unroll
    for (int u = 0; u < 4; ++u) {
      X[u] = (tau & 2) ? ((tau & 1) ? d[(3 - u) & 3][u] : d[(2 - u) & 3][u]) : ((tau & 1) ? d[(1 - u) & 3][u] : d[(0 - u) & 3][u]);
      Y[u] = (tau & 2) ? ((tau & 1) ? e[(3 - u) & 3][u] : e[(2 - u) & 3][u]) : ((tau & 1) ? e[(1 - u) & 3][u] : e[(0 - u) & 3][u]);
    }
    const float s0 = (X[0] + dpp<0x12C>(X[1])) + (dpp<0x128>(X[2]) + dpp<0x124>(X[3]));
    const float s1 = (Y[0] + dpp<0x12C>(Y[1])) + (dpp<0x128>(Y[2]) + dpp<0x124>(Y[3]));
    mx = fmaxf(mx, fmaxf(s0, s1));
    *(LAS f32x2*)(lds + sadr + st * 512) = f32x2{s0, s1};
    SB();
  };
#pragma unroll 1
    for (int it = 0; it < 3; ++it) {
      const bool kk = it < 2;
      const rsrc_t rr = mkrsrc(kk ? kp : vp, SK * 256);
      const int so0 = kk ? w0 + (it + 1) * 4 * STG : u0;
      unsigned xo = kk ? k2o : vvo;
      scoreK(I0{}); ISSUE(0, rr, xo, so0, KSP); scoreK(I1{}); ISSUE(1, rr, xo, so0 + STG, KSP); scoreK(I2{}); ISSUE(2, rr, xo, so0 + 2 * STG, KSP); scoreK(I3{}); ISSUE(3, rr, xo, so0 + 3 * STG, KSP);
      sadr += 2048;
    }
    mx = fmaxf(mx, dpp<0x124>(mx)); mx = fmaxf(mx, dpp<0x128>(mx)); mx = xrow_max(mx);
    WAITF(F_HM, 4, 1);
    mx = fmaxf(mx, *(LAS float*)(lds + L_HM + w4 * 16 + (c & 3) * 4));
    const float mc = mx * C2;
    float l0 = 0.f, l1 = 0.f;
#pragma unroll
    for (int sg = 0; sg < 16; ++sg) {
      const f32x2 sc = *(LAS f32x2*)(lds + L_S + w4 * 8192 + sg * 512 + v_l8);
      const float p0 = __builtin_amdgcn_exp2f(fmaf(sc[0], C2, -mc)), p1 = __builtin_amdgcn_exp2f(fmaf(sc[1], C2, -mc));
      l0 += p0; l1 += p1;
      *(LAS unsigned*)(lds + L_P + w4 * 4096 + sg * 256 + g * 64 + (c & 3) * 16 + tau * 4) = cvtpk(p0, p1);
    }
    float l = l0 + l1;
    l += dpp<0x128>(l); l += dpp<0x124>(l); l = xrow_sum(l);
    if (lane < 4) *(LAS f32x2*)(lds + L_ML + lane * 32 + w4 * 8) = f32x2{mc, l};
    BUMP(F_L);
    f32x4 acc[8];
#pragma unroll
    for (int i = 0; i < 8; ++i) acc[i] = f32x4{0.f, 0.f, 0.f, 0.f};
    unsigned padr = L_P + w4 * 4096 + g * 64 + (c & 3) * 16;
    auto pv = [&](auto stc) __attribute__((always_inline)) {
      constexpr int st = decltype(stc)::value;
      const u32x4 pf = *(LAS u32x4*)(lds + padr + st * 256);
#pragma unroll
      for (int w = 0; w < 4; ++w) {
        u32x4 a0, a1;
#pragma unroll
        for (int a = 0; a < 4; ++a) { a0[a] = __builtin_amdgcn_perm(bb[st][a][w], ba[st][a][w], PLO); a1[a] = __builtin_amdgcn_perm(bb[st][a][w], ba[st][a][w], PHI); }
        acc[2 * w] = mfma16(a0, pf, acc[2 * w]);
        acc[2 * w + 1] = mfma16(a1, pf, acc[2 * w + 1]);
      }
      SB();
    };
#pragma unroll 1
    for (int it = 0; it < 3; ++it) {
      const int so0 = u0 + (it + 1) * 4 * STG;
      pv(I0{}); ISSUE(0, rv, vvo, so0, VSP); pv(I1{}); ISSUE(1, rv, vvo, so0 + STG, VSP); pv(I2{}); ISSUE(2, rv, vvo, so0 + 2 * STG, VSP); pv(I3{}); ISSUE(3, rv, vvo, so0 + 3 * STG, VSP);
      padr += 1024;
    }
    WAITF(F_F, 4, 1);
    const f32x4 fa = *(LAS f32x4*)(lds + L_FA + g * 16);
    pv(I0{}); pv(I1{}); pv(I2{}); pv(I3{});
    if (c < 4) {
#pragma unroll
      for (int i = 0; i < 8; ++i) *(LAS f32x4*)(lds + v_dep + i * 16) = acc[i];
    }
    CFENCE();
    unsigned arr = 0;
    if (lane == 0) arr = LINC(F_MERGE);
    const unsigned arru = __builtin_amdgcn_readfirstlane(arr);
    const bool last = arru == 3;
    CFENCE();
    TS(3);
    if (last) {
      f32x4 o0 = {0.f, 0.f, 0.f, 0.f}, o1 = {0.f, 0.f, 0.f, 0.f};
#pragma unroll
      for (int w = 0; w < 4; ++w) { o0 += fa[w] * *(LAS f32x4*)(lds + v_recd + w * 4 * MREC); o1 += fa[w] * *(LAS f32x4*)(lds + v_recd + w * 4 * MREC + 16); }
      u32x4 d0 = {cvtpk(o0[0], o1[0]), tag, cvtpk(o0[1], o1[1]), tag}, d1 = {cvtpk(o0[2], o1[2]), tag, cvtpk(o0[3], o1[3]), tag};
      asm volatile("" : "+v"(d0), "+v"(d1));
      __builtin_amdgcn_raw_buffer_store_b128(d0, rws, v_ost, s_ost, AUX_SC1);
      __builtin_amdgcn_raw_buffer_store_b128(d1, rws, v_ost + 128, s_ost, AUX_SC1);
      TS(4);
    }
    } else {
    u32x4 ba[4][4], bb[4][4];
    ISSUE(0, rk, k2o, w0, HSP);
    __builtin_amdgcn_s_barrier(); CFENCE();
    SB();
    ISSUE(1, rk, k2o, w0 + STG, HSP); ISSUE(2, rk, k2o, w0 + 2 * STG, HSP); ISSUE(3, rk, k2o, w0 + 3 * STG, HSP);
    tag = __builtin_amdgcn_readfirstlane(ep) + 0x9E3779B9u;
    TS(1);
    u32x4 qg[2];
#pragma unroll 1
    for (unsigned spin = 0; spin < SPIN_CAP; ++spin) {
      CFENCE();
      qg[0] = __builtin_amdgcn_raw_buffer_load_b128(rws, lane * 16, WS_QG + (kvh * 8 + b) * QS + w4 * 2048, AUX_SC1);
      qg[1] = __builtin_amdgcn_raw_buffer_load_b128(rws, lane * 16 + 1024, WS_QG + (kvh * 8 + b) * QS + w4 * 2048, AUX_SC1);
      if (__all((qg[0][1] == tag) & (qg[0][3] == tag) & (qg[1][1] == tag) & (qg[1][3] == tag))) break;
    }
    const float a0 = bflo(qg[0][0]) + bflo(qg[1][0]), a1 = bfhi(qg[0][0]) + bfhi(qg[1][0]), b0 = bflo(qg[0][2]) + bflo(qg[1][2]), b1 = bfhi(qg[0][2]) + bfhi(qg[1][2]);
    auto t0 = __builtin_amdgcn_permlane32_swap(__float_as_uint(a0), __float_as_uint(b0), false, false);
    auto t1 = __builtin_amdgcn_permlane32_swap(__float_as_uint(a1), __float_as_uint(b1), false, false);
    const int p = 2 * c + (lane >> 5);
    *(LAS unsigned*)(lds + L_Q + w4 * 256 + (g & 1) * 128 + (p >> 3) * 32 + (p & 3) * 8 + ((p >> 2) & 1) * 4) =
        cvtpk(__uint_as_float(t0[0]) + __uint_as_float(t0[1]), __uint_as_float(t1[0]) + __uint_as_float(t1[1]));
    BUMP(F_Q);
  v_recd = L_M + g * MREC + c * 32, v_dep = L_M + (w4 * 4 + c) * MREC + g * 128;
  v_ost = (lane >> 3) * OS + (lane & 7) * 16;
  v_wld = (32 * (g & 1) + 16 * (g >> 1)) * 1024 + c * 8;
  v_yst = (g + (c >> 3)) * 512 + (c & 7) * 16, v_l8 = lane * 8;
  s_ost = RFL(WS_OG + kvh * 8 * OS + (b * 4 + s) * 256), s_wld = RFL((64 * (w4 >> 1) + 4 * (w4 & 1)) * 1024), s_old = RFL(WS_OG + (H * 2) * OS + wave * 1024);
  s_yst = RFL(WS_QG + (kvh * 8 + wave) * QS + b * 2048 + s * 128), s_yld = RFL(WS_QG + (wave * 8 + b) * QS + j * 512);
  s_out = RFL(b * 1024 + (j >> 2) * 128 + (j & 1) * 64 + ((j >> 1) & 1) * 4);
  wtile = wo + (size_t)H * 131072 + b * 128;
  tag = RFL(tag);
  asm volatile("" : "+v"(v_recd), "+v"(v_dep), "+v"(v_ost), "+v"(v_wld), "+v"(v_yst), "+v"(v_l8));
  asm volatile("" : "+s"(s_ost), "+s"(s_wld), "+s"(s_old), "+s"(s_yst), "+s"(s_yld), "+s"(s_out), "+s"(wtile));
  WAITF(F_Q, 4, 1);
  TS(2);

  u32x4 qf[4];
#pragma unroll
  for (int t = 0; t < 4; ++t) qf[t] = *(LAS u32x4*)(lds + L_Q + (c & 3) * 256 + (4 * g + ((tau - t) & 3)) * 16);
  float mx = -INFINITY;
  unsigned sadr = L_S + w4 * 8192 + (wave >> 2) * 2048 + lane * 8;
  auto scoreK = [&](auto stc) __attribute__((always_inline)) {
    constexpr int st = decltype(stc)::value;
    f32x4 d[4], e[4];
#pragma unroll
    for (int i = 0; i < 4; ++i) d[i] = mfma16(ba[st][i], qf[i], f32x4{0.f, 0.f, 0.f, 0.f});
#pragma unroll
    for (int i = 0; i < 4; ++i) e[i] = mfma16(bb[st][i], qf[i], f32x4{0.f, 0.f, 0.f, 0.f});
    float X[4], Y[4];
#pragma unroll
    for (int u = 0; u < 4; ++u) {
      X[u] = (tau & 2) ? ((tau & 1) ? d[(3 - u) & 3][u] : d[(2 - u) & 3][u]) : ((tau & 1) ? d[(1 - u) & 3][u] : d[(0 - u) & 3][u]);
      Y[u] = (tau & 2) ? ((tau & 1) ? e[(3 - u) & 3][u] : e[(2 - u) & 3][u]) : ((tau & 1) ? e[(1 - u) & 3][u] : e[(0 - u) & 3][u]);
    }
    const float s0 = (X[0] + dpp<0x12C>(X[1])) + (dpp<0x128>(X[2]) + dpp<0x124>(X[3]));
    const float s1 = (Y[0] + dpp<0x12C>(Y[1])) + (dpp<0x128>(Y[2]) + dpp<0x124>(Y[3]));
    mx = fmaxf(mx, fmaxf(s0, s1));
    *(LAS f32x2*)(lds + sadr + st * 512) = f32x2{s0, s1};
    SB();
  };
    scoreK(I0{}); scoreK(I1{}); scoreK(I2{}); scoreK(I3{});
    mx = fmaxf(mx, dpp<0x124>(mx)); mx = fmaxf(mx, dpp<0x128>(mx)); mx = xrow_max(mx);
    if (lane < 4) *(LAS float*)(lds + L_HM + w4 * 16 + lane * 4) = mx;
    BUMP(F_HM);
    TS(3);
    if (wave == 0) {
      WAITF(F_L, 4, 16);
      const f32x2 ml = *(LAS f32x2*)(lds + L_ML + c * 8);
      float M = fmaxf(ml[0], dpp<0xB1>(ml[0])); M = fmaxf(M, dpp<0x4E>(M));
      float L = ml[1] * __builtin_amdgcn_exp2f(ml[0] - M); L += dpp<0xB1>(L); L += dpp<0x4E>(L);
      const unsigned lse = __float_as_uint(M + __builtin_amdgcn_logf(L));
      const unsigned lbase = WS_LG + (b * 8 + kvh) * 512;
      if (lane < 16) __builtin_amdgcn_raw_buffer_store_b64(u32x2{lse, tag}, rws, lane * 8, lbase + s * 128, AUX_SC1);
      u32x4 lg;
#pragma unroll 1
      for (unsigned spin = 0; spin < SPIN_CAP; ++spin) {
        __builtin_amdgcn_s_sleep(LXS); __builtin_amdgcn_s_sleep(LXS);
        CFENCE();
        lg = u32x4{0u, tag, 0u, tag};
        if (lane < 16) lg = __builtin_amdgcn_raw_buffer_load_b128(rws, (lane & 3) * 128 + (lane >> 2) * 32, lbase, AUX_SC1);
        if (__all((lg[1] == tag) & (lg[3] == tag))) break;
      }
      const float ls = __uint_as_float(lg[0]);
      float G = fmaxf(ls, dpp<0xB1>(ls)); G = fmaxf(G, dpp<0x4E>(G));
      float E = __builtin_amdgcn_exp2f(ls - G); E += dpp<0xB1>(E); E += dpp<0x4E>(E);
      if (lane < 16) *(LAS float*)(lds + L_FA + lane * 4) = __builtin_amdgcn_exp2f(ml[0] - G) * __builtin_amdgcn_rcpf(E);
    }
    BUMP(F_F);
  }
  tag = RFL(tag); s_old = RFL(s_old); s_yst = RFL(s_yst); s_yld = RFL(s_yld); s_out = RFL(s_out); s_wld = RFL(s_wld);
  if (wave < 4) {
    WAITF(F_MERGE, 1, 16);
    WAITF(F_MERGE, WTRIG, 2);
    const rsrc_t rw = mkrsrc(wtile, 131072);
    u32x2 wr[8];
#pragma unroll
    for (int i = 0; i < 8; ++i) wr[i] = __builtin_amdgcn_raw_buffer_load_b64(rw, v_wld + (2 * (i >> 2) + (i & 1)) * 1024, s_wld + 8 * ((i >> 1) & 1) * 1024, 0);
#pragma unroll
    for (int w = 0; w < 2; ++w) {
      u32x4 f0, f1;
#pragma unroll
      for (int a = 0; a < 4; ++a) { f0[a] = __builtin_amdgcn_perm(wr[2 * a + 1][w], wr[2 * a][w], PLO); f1[a] = __builtin_amdgcn_perm(wr[2 * a + 1][w], wr[2 * a][w], PHI); }
      *(LAS u32x4*)(lds + L_W + (w4 * 4 + 2 * w) * 1024 + vvo) = f0; *(LAS u32x4*)(lds + L_W + (w4 * 4 + 2 * w + 1) * 1024 + vvo) = f1;
    }
    BUMP(F_W);
  }
  WAITF(F_W, 4, 2);
  u32x4 wf[16], og0, og1;
#pragma unroll
  for (int i = 0; i < 16; ++i) wf[i] = *(LAS u32x4*)(lds + L_W + i * 1024 + vvo);
  WAITF(F_MERGE, 4, 2);
  {
    bool no = true;
#pragma unroll 1
    for (unsigned spin = 0; spin < SPIN_CAP; ++spin) {
      CFENCE();
      if (no) {
        og0 = __builtin_amdgcn_raw_buffer_load_b128(rws, k2o, s_old, AUX_SC1);
        og1 = __builtin_amdgcn_raw_buffer_load_b128(rws, k2o, s_old + OS, AUX_SC1);
        no = !((og0[1] == tag) & (og0[3] == tag) & (og1[1] == tag) & (og1[3] == tag));
      }
      if (!__any(no)) break;
      __builtin_amdgcn_s_sleep(PSLEEP);
    }
  }
  TS(5);
  {
    const int u = c & 3;
    const u32x4 a0 = {u == 0 ? og0[0] : 0u, u == 0 ? og0[2] : 0u, u == 1 ? og0[0] : 0u, u == 1 ? og0[2] : 0u};
    const u32x4 a1 = {u == 2 ? og0[0] : 0u, u == 2 ? og0[2] : 0u, u == 3 ? og0[0] : 0u, u == 3 ? og0[2] : 0u};
    const u32x4 a2 = {u == 0 ? og1[0] : 0u, u == 0 ? og1[2] : 0u, u == 1 ? og1[0] : 0u, u == 1 ? og1[2] : 0u};
    const u32x4 a3 = {u == 2 ? og1[0] : 0u, u == 2 ? og1[2] : 0u, u == 3 ? og1[0] : 0u, u == 3 ? og1[2] : 0u};
    f32x4 d[4];
#pragma unroll
    for (int i = 0; i < 4; ++i) d[i] = mfma16(a0, wf[i], f32x4{0.f, 0.f, 0.f, 0.f});
#pragma unroll
    for (int i = 0; i < 4; ++i) d[i] = mfma16(a1, wf[4 + i], d[i]);
#pragma unroll
    for (int i = 0; i < 4; ++i) d[i] = mfma16(a2, wf[8 + i], d[i]);
#pragma unroll
    for (int i = 0; i < 4; ++i) d[i] = mfma16(a3, wf[12 + i], d[i]);
    float y[4];
#pragma unroll
    for (int i = 0; i < 4; ++i) y[i] = (d[i][0] + d[i][1]) + (d[i][2] + d[i][3]);
#pragma unroll
    for (int i = 0; i < 2; ++i) { auto t = __builtin_amdgcn_permlane32_swap(__float_as_uint(y[i]), __float_as_uint(y[i + 2]), false, false); y[i] = __uint_as_float(t[0]) + __uint_as_float(t[1]); }
#pragma unroll
    for (int i = 0; i < 2; ++i) { auto t = __builtin_amdgcn_permlane16_swap(__float_as_uint(y[i]), __float_as_uint(y[i]), false, false); y[i] = __uint_as_float(t[0]) + __uint_as_float(t[1]); }
    if ((g & 1) == 0) __builtin_amdgcn_raw_buffer_store_b128(u32x4{__float_as_uint(y[0]), tag + 1u, __float_as_uint(y[1]), tag + 1u}, rws, v_yst, s_yst, AUX_SC1);
  }
  TS(6);
  u32x4 yg;
  {
    bool ny = lane < 32;
#pragma unroll 1
    for (unsigned spin = 0; spin < SPIN_CAP; ++spin) {
      CFENCE();
      if (ny) { yg = __builtin_amdgcn_raw_buffer_load_b128(rws, vvo, s_yld, AUX_SC1); ny = !((yg[1] == tag + 1u) & (yg[3] == tag + 1u)); }
      if (!__any(ny)) break;
      __builtin_amdgcn_s_sleep(PSLEEP2);
    }
  }
  if (lane < 32) *(LAS u32x2*)(lds + L_R + wave * 256 + v_l8) = u32x2{yg[0], yg[2]};
  CFENCE();
  unsigned arr2 = 0;
  if (lane == 0) arr2 = LINC(F_OUT);
  if (__builtin_amdgcn_readfirstlane(arr2) == NW - 1) {
    CFENCE();
    f32x2 p[4];
#pragma unroll
    for (int w = 0; w < 4; ++w) p[w] = *(LAS f32x2*)(lds + L_R + w * 512 + v_l8);
    const f32x2 ps = (p[0] + p[1]) + (p[2] + p[3]);
    float r0 = ps[0], r1 = ps[1];
    r0 += dpp<0x128>(r0); r1 += dpp<0x128>(r1);
    r0 = xrow_sum(r0); r1 = xrow_sum(r1);
    if (lane < 8) __builtin_amdgcn_raw_buffer_store_b32(cvtpk(r0, r1), mkrsrc(out, 8192), v_l8, s_out, 0);
    if (lane == 0 && blk == 0) __builtin_amdgcn_raw_buffer_store_b32(tag, rws, 0, WS_EPOCH, AUX_SC1);
    TS(7);
  }
}

extern "C" void kernel_launch(void* const* d_in, const int* in_sizes, int n_in, void* d_out, int out_size, void* d_ws, size_t ws_size, hipStream_t stream) {
  fused_gqa<<<256, NTHR, 0, stream>>>((const unsigned char*)d_in[0], (const unsigned char*)d_in[1], (const unsigned char*)d_in[2], (const unsigned char*)d_in[3], (const unsigned char*)d_in[4],
                                      (unsigned char*)d_out, (unsigned char*)d_ws);
}
```

```cpp
#include <hip/hip_runtime.h>
#include <hip/hip_bf16.h>

namespace {

constexpr int SK = 8192, NTHR = 512;
constexpr float C2 = 0.088388347648318440f * 1.4426950408889634f;
constexpr unsigned SPIN_CAP = 1u << 20;
constexpr unsigned OOB = 0x40000000u;
constexpr int KSP = 4;
constexpr int MARGIN = 40;
constexpr int PACE = 1030;
constexpr int WOFF = 108;
constexpr int LXS = 56;
constexpr int RDLY = 12;
constexpr int PSLEEP = 4, PSLEEP2 = 1;

using bf16x8 = __attribute__((ext_vector_type(8))) __bf16;
using bf16x2 = __attribute__((ext_vector_type(2))) __bf16;
using u32x4  = __attribute__((ext_vector_type(4))) unsigned;
using u32x2  = __attribute__((ext_vector_type(2))) unsigned;
using f32x4  = __attribute__((ext_vector_type(4))) float;
using f32x2  = __attribute__((ext_vector_type(2))) float;
using rsrc_t = __amdgpu_buffer_rsrc_t;
#define LAS __attribute__((address_space(3)))

__device__ __forceinline__ unsigned cvtpk(float lo, float hi) { return __builtin_bit_cast(unsigned, __builtin_convertvector(f32x2{lo, hi}, bf16x2)); }
__device__ __forceinline__ u32x2 pack21(float a, float b, unsigned tag) {
  return u32x2{((__float_as_uint(a) + 0x400u) & ~0x7ffu) | (tag & 0x7ffu), ((__float_as_uint(b) + 0x400u) & ~0x7ffu) | ((tag >> 11) & 0x7ffu)};
}
__device__ __forceinline__ float val21(unsigned u) { return __uint_as_float(u & ~0x7ffu); }
__device__ __forceinline__ bool ok21(u32x4 v, unsigned tag) { return (((v[0] ^ tag) | (v[2] ^ tag) | (v[1] ^ (tag >> 11)) | (v[3] ^ (tag >> 11))) & 0x7ffu) == 0u; }
__device__ __forceinline__ f32x4 mfma16(u32x4 a, u32x4 b, f32x4 c) {
  return __builtin_amdgcn_mfma_f32_16x16x32_bf16(__builtin_bit_cast(bf16x8, a), __builtin_bit_cast(bf16x8, b), c, 0, 0, 0);
}
template <int CTRL> __device__ __forceinline__ float dpp(float x) {
  return __builtin_bit_cast(float, __builtin_amdgcn_mov_dpp(__builtin_bit_cast(int, x), CTRL, 0xf, 0xf, true));
}
constexpr int XOR1 = 0xB1, XOR2 = 0x4E, ROR4 = 0x124, ROR8 = 0x128, ROR12 = 0x12C;
__device__ __forceinline__ float xrow_max(float x) {
  auto s = __builtin_amdgcn_permlane16_swap(__float_as_uint(x), __float_as_uint(x), false, false);
  x = fmaxf(__uint_as_float(s[0]), __uint_as_float(s[1]));
  auto t = __builtin_amdgcn_permlane32_swap(__float_as_uint(x), __float_as_uint(x), false, false);
  return fmaxf(__uint_as_float(t[0]), __uint_as_float(t[1]));
}
__device__ __forceinline__ float xrow_sum(float x) {
  auto s = __builtin_amdgcn_permlane16_swap(__float_as_uint(x), __float_as_uint(x), false, false);
  x = __uint_as_float(s[0]) + __uint_as_float(s[1]);
  auto t = __builtin_amdgcn_permlane32_swap(__float_as_uint(x), __float_as_uint(x), false, false);
  return __uint_as_float(t[0]) + __uint_as_float(t[1]);
}
__device__ __forceinline__ rsrc_t mkrsrc(const void* p, unsigned bytes) { return __builtin_amdgcn_make_buffer_rsrc(const_cast<void*>(p), 0, bytes, 0x00020000); }
__device__ __forceinline__ unsigned plo(unsigned r0, unsigned r1) { return __builtin_amdgcn_perm(r1, r0, 0x05040100u); }
__device__ __forceinline__ unsigned phi(unsigned r0, unsigned r1) { return __builtin_amdgcn_perm(r1, r0, 0x07060302u); }
__device__ __forceinline__ float sel4(int t, float a0, float a1, float a2, float a3) { const float lo = (t & 1) ? a1 : a0, hi = (t & 1) ? a3 : a2; return (t & 2) ? hi : lo; }
__device__ __forceinline__ float unit_score(f32x4 d0, f32x4 d1, f32x4 d2, f32x4 d3, int tau) {
  const float x0 = sel4(tau, d0[0], d1[0], d2[0], d3[0]), x1 = sel4(tau, d3[1], d0[1], d1[1], d2[1]);
  const float x2 = sel4(tau, d2[2], d3[2], d0[2], d1[2]), x3 = sel4(tau, d1[3], d2[3], d3[3], d0[3]);
  return (x0 + dpp<ROR12>(x1)) + (dpp<ROR8>(x2) + dpp<ROR4>(x3));
}
#define CFENCE() asm volatile("" ::: "memory")
#define SB() __builtin_amdgcn_sched_barrier(0)
#define RFL(x) (unsigned)__builtin_amdgcn_readfirstlane((int)(x))
constexpr int AUX_NT = 2, AUX_SC1 = 16;

constexpr unsigned QS = 8192 + 256, OS = 8192 + 256;
constexpr unsigned WS_EPOCH = 0;
constexpr unsigned WS_QG = 4096;
constexpr unsigned WS_OG = WS_QG + 64 * QS;
constexpr unsigned WS_LG = WS_OG + 64 * OS;
constexpr unsigned WS_TG = WS_LG + 64 * 512;
constexpr unsigned WS_END = WS_TG + 16 * 128;
constexpr int MREC = 512 + 16;
constexpr int L_M = 0;
constexpr int L_QT = L_M + 16 * MREC;
constexpr int L_Q = L_QT + 8192;
constexpr int L_F = L_Q + 1024;
constexpr int L_HM = L_F + 64;
constexpr int L_ML = L_HM + 64;
constexpr int L_FA = L_ML + 128;
constexpr int L_T = L_FA + 64;
constexpr int L_S = L_T + 32;
constexpr int L_P = L_S + 32768;
constexpr int L_W = L_P + 16384;
constexpr int L_END = L_W + 16384;
static_assert(L_END > 80 * 1024 && L_END <= 160 * 1024, "one workgroup per CU");
enum { F_QP = 0, F_Q, F_P, F_L, F_F, F_MERGE, F_W, F_TAG, F_REF = 8  , F_CNT = 10, F_T, F_C, F_K };
constexpr int STG = 65536, UB = 16 * STG;

__global__ void __launch_bounds__(NTHR, 2) fused_gqa(const unsigned char* __restrict__ x, const unsigned char* __restrict__ kc, const unsigned char* __restrict__ vc,
                                                      const unsigned char* __restrict__ wq, const unsigned char* __restrict__ wo, unsigned char* __restrict__ out, unsigned char* ws) {
  __shared__ __attribute__((aligned(16))) unsigned char lds_raw[L_END];
  LAS unsigned char* lds = (LAS unsigned char*)lds_raw;
  volatile LAS unsigned* flg = (volatile LAS unsigned*)(lds + L_F);
  const int blk = blockIdx.x, j = blk & 31, b = blk >> 5, kvh = j & 7, s = j >> 3, H = kvh * 4 + s;
  const int tid = threadIdx.x, lane = tid & 63, wave = __builtin_amdgcn_readfirstlane(tid >> 6), g = lane >> 4, c = lane & 15, w4 = wave & 3, tau = c >> 2;
  const int sidx = blk >> 4;
  const bool sampled = (blk & 15) == (sidx & 7);
  unsigned k2o = tau * 256 + g * 64 + (c & 3) * 16, vvo = lane * 16;
  int u0 = (s * 4 + w4) * 4096, w0 = u0 + (wave >> 2) * 4 * STG;
  size_t kvoff = (size_t)(b * 8 + kvh) * SK * 256;
  asm volatile("" : "+v"(k2o), "+v"(vvo), "+s"(u0), "+s"(w0), "+s"(kvoff));
  SB();
  asm volatile("" : "+s"(x), "+s"(kc), "+s"(vc), "+s"(wq), "+s"(wo), "+s"(out), "+s"(ws));
  const unsigned char* kp = kc + kvoff; const unsigned char* vp = vc + kvoff;
  const rsrc_t rk = mkrsrc(kp, SK * 256), rv = mkrsrc(vp, SK * 256), rws = mkrsrc(ws, WS_END);

#define LINC(f) __builtin_amdgcn_atomic_inc32((LAS unsigned*)(lds + L_F + 4 * (f)), 0xffffffffu, __ATOMIC_RELAXED, "workgroup")
#define BUMP(f) do { CFENCE(); if (lane == 0) LINC(f); } while (0)
#define WAITF(f, n, slp) do { _Pragma("unroll 1") for (unsigned sp_ = 0; sp_ < (SPIN_CAP << 5); ++sp_) { if (flg[f] >= (unsigned)(n)) break; __builtin_amdgcn_s_sleep(slp); } CFENCE(); } while (0)
#define ISSUE(st, rs, vo, so, nsp) do { int sa_ = (so), sb_ = (so) + UB; asm volatile("" : "+v"(vo), "+s"(sa_), "+s"(sb_)); SB(); \
    _Pragma("unroll") for (int t_ = 0; t_ < 4; ++t_) { \
      if (t_ < (nsp)) { const u32x2 lo_ = __builtin_amdgcn_raw_buffer_load_b64(rs, vo + t_ * 1024, sa_, AUX_NT); SB(); \
                        const u32x2 hi_ = __builtin_amdgcn_raw_buffer_load_b64(rs, vo + t_ * 1024 + 8, sa_, AUX_NT); SB(); \
                        ba[st][t_] = u32x4{lo_[0], lo_[1], hi_[0], hi_[1]}; } \
      else { ba[st][t_] = __builtin_amdgcn_raw_buffer_load_b128(rs, vo + t_ * 1024, sa_, AUX_NT); SB(); } } \
    _Pragma("unroll") for (int t_ = 0; t_ < 4; ++t_) { bb[st][t_] = __builtin_amdgcn_raw_buffer_load_b128(rs, vo + t_ * 1024, sb_, AUX_NT); SB(); } \
    } while (0)
#define STEP_V(st, so) do { \
    const u32x2 ref_ = *(volatile LAS u32x2*)(lds + L_F + 4 * F_REF); \
    unsigned cnt_ = 0; if (lane == 0) cnt_ = LINC(F_CNT); \
    const unsigned now_ = (unsigned)__builtin_amdgcn_s_memrealtime(), r0_ = RFL(ref_[0]), r1_ = RFL(ref_[1]), n_ = RFL(cnt_); \
    const bool late_ = (r1_ != 0u) & ((int)(now_ - (r0_ + ((n_ * r1_) >> 6))) > 0); \
    const rsrc_t rd_ = mkrsrc(vp, late_ ? 0u : SK * 256u); \
    PV(st); \
    int sa_ = (so), sb_ = (so) + UB; asm volatile("" : "+v"(vvo), "+s"(sa_), "+s"(sb_)); SB(); \
    _Pragma("unroll") for (int t_ = 0; t_ < 4; ++t_) { \
      ba[st][t_] = __builtin_amdgcn_raw_buffer_load_b128(rv, vvo + t_ * 1024, sa_, AUX_NT); SB(); \
      dup[st][t_] = __builtin_amdgcn_raw_buffer_load_b32(rd_, vvo + t_ * 1024, sa_, AUX_NT); SB(); } \
    _Pragma("unroll") for (int t_ = 0; t_ < 4; ++t_) { bb[st][t_] = __builtin_amdgcn_raw_buffer_load_b128(rv, vvo + t_ * 1024, sb_, AUX_NT); SB(); } \
    } while (0)
#define SCORE(st) do { f32x4 d_[4], e_[4]; \
    _Pragma("unroll") for (int i_ = 0; i_ < 4; ++i_) d_[i_] = mfma16(ba[st][i_], qf[i_], f32x4{0.f, 0.f, 0.f, 0.f}); \
    _Pragma("unroll") for (int i_ = 0; i_ < 4; ++i_) e_[i_] = mfma16(bb[st][i_], qf[i_], f32x4{0.f, 0.f, 0.f, 0.f}); \
    const float s0_ = unit_score(d_[0], d_[1], d_[2], d_[3], tau), s1_ = unit_score(e_[0], e_[1], e_[2], e_[3], tau); \
    *(LAS f32x2*)(lds + sadr + (st) * 512) = f32x2{s0_, s1_}; SB(); } while (0)
  unsigned v_yst, v_yld, v_l8, v_out, s_old, s_yst, s_yld, s_out, tag;
#define TAIL_ADDR_AND_Q() \
    v_yst = (g >> 1) * 512 + c * 8; v_yld = (lane >> 5) * 8 * QS + (lane & 31) * 16; v_l8 = lane * 8; v_out = (lane & 7) * 16 + (lane >> 5) * 8; \
    s_old = RFL(WS_OG + (H * 2) * OS + wave * 1024); s_yst = RFL(WS_QG + (kvh * 8 + wave) * QS + b * 1024 + s * 128); \
    s_yld = RFL(WS_QG + b * QS + j * 512); s_out = RFL(b * 1024 + (j >> 1) * 128 + (j & 1) * 4); \
    asm volatile("" : "+v"(v_yst), "+v"(v_yld), "+v"(v_l8), "+v"(v_out), "+s"(s_old), "+s"(s_yst), "+s"(s_yld), "+s"(s_out)); \
    WAITF(F_Q, 4, 1); \
    u32x4 qf[4]; \
    _Pragma("unroll") for (int t_ = 0; t_ < 4; ++t_) qf[t_] = *(LAS u32x4*)(lds + L_Q + (c & 3) * 256 + (4 * g + ((tau - t_) & 3)) * 16); \
    unsigned sadr = L_S + w4 * 8192 + (wave >> 2) * 2048 + lane * 8

  if (wave >= 4) {
    u32x4 ba[4][4], bb[4][4];
    __builtin_amdgcn_s_setprio(3);
    const unsigned ep = __builtin_amdgcn_raw_buffer_load_b32(rws, 0, wave == 4 ? WS_EPOCH : OOB, AUX_SC1);
    SB();
    const rsrc_t rx = mkrsrc(x, 8192), rq = mkrsrc(wq + (size_t)(s * 128 + w4 * 32) * 8192 + (kvh * 8 + b) * 128, 32 * 8192);
    const u32x4 xf = __builtin_amdgcn_raw_buffer_load_b128(rx, (c & 7) * 1024 + g * 16, s * 256 + w4 * 64, 0);
    u32x2 wr[8];
#pragma unroll
    for (int i = 0; i < 8; ++i) wr[i] = __builtin_amdgcn_raw_buffer_load_b64(rq, g * 65536 + c * 8, i * 8192, AUX_NT);
    const unsigned pa = WS_OG + (kvh * 8 + w4 * 2) * OS + (b * 4 + s) * 256 + (lane >> 1) * OS + (lane & 1) * 128;
    const unsigned pc = lane != 4 ? OOB : w4 == 1 ? WS_LG + b * 4096 + (kvh * 4 + s) * 128 : (w4 == 2 && sampled) ? WS_TG + sidx * 128 : OOB;
    const unsigned pw = __builtin_amdgcn_raw_buffer_load_b32(rws, lane < 4 ? pa : pc, 0, AUX_SC1);
    SB();
    if (tid < 256 + 16) flg[tid - 256] = 0u;
    asm volatile("s_waitcnt lgkmcnt(0)" ::: "memory"); __builtin_amdgcn_s_barrier(); CFENCE();
    SB();
    if (tid == 256) flg[F_TAG] = ep + 0x9E3779B9u;
#pragma unroll
    for (int w = 0; w < 2; ++w) {
      u32x4 a0, a1;
#pragma unroll
      for (int a = 0; a < 4; ++a) { a0[a] = plo(wr[2 * a][w], wr[2 * a + 1][w]); a1[a] = phi(wr[2 * a][w], wr[2 * a + 1][w]); }
      const f32x4 d0 = mfma16(a0, xf, f32x4{0.f, 0.f, 0.f, 0.f}), d1 = mfma16(a1, xf, f32x4{0.f, 0.f, 0.f, 0.f});
      if (c < 8) {
        *(LAS f32x4*)(lds + L_QT + w4 * 2048 + c * 256 + g * 64 + (2 * w) * 16) = d0;
        *(LAS f32x4*)(lds + L_QT + w4 * 2048 + c * 256 + g * 64 + (2 * w + 1) * 16) = d1;
      }
    }
    BUMP(F_QP);
    WAITF(F_QP, 4, 0);
    tag = RFL(flg[F_TAG]);
    {
      const int p = lane & 31, ro = (2 * w4 + (lane >> 5)) * 256 + (p >> 3) * 64 + ((p >> 2) & 1) * 32 + (p & 3) * 4;
      float q0[4], q1[4];
#pragma unroll
      for (int w = 0; w < 4; ++w) { q0[w] = *(LAS float*)(lds + L_QT + w * 2048 + ro); q1[w] = *(LAS float*)(lds + L_QT + w * 2048 + ro + 16); }
      const float t0 = (q0[0] + q0[1]) + (q0[2] + q0[3]), t1 = (q1[0] + q1[1]) + (q1[2] + q1[3]);
      __builtin_amdgcn_raw_buffer_store_b64(pack21(t0, t1, tag), rws, (lane >> 5) * QS + p * 8,
                                            WS_QG + (kvh * 8 + 2 * w4) * QS + (b >> 1) * 2048 + s * 512 + (b & 1) * 256, AUX_SC1);
    }
    asm volatile("" :: "v"(pw));
    SB();
    __builtin_amdgcn_s_setprio(0);
    ISSUE(0, rk, k2o, w0, KSP); ISSUE(1, rk, k2o, w0 + STG, KSP); ISSUE(2, rk, k2o, w0 + 2 * STG, KSP); ISSUE(3, rk, k2o, w0 + 3 * STG, KSP);
    unsigned v_rec = L_M + g * MREC + c * 32, v_dep = L_M + (w4 * 4 + c) * MREC + g * 128, v_ost = (lane >> 3) * OS + (lane & 7) * 16;
    unsigned s_ost = RFL(WS_OG + kvh * 8 * OS + (b * 4 + s) * 256);
    asm volatile("" : "+v"(v_rec), "+v"(v_dep), "+v"(v_ost), "+s"(s_ost));
    const unsigned t_a = (unsigned)__builtin_amdgcn_s_memrealtime();
    TAIL_ADDR_AND_Q();
#pragma unroll 1
    for (int it = 0; it < 3; ++it) {
      const bool kk = it < 2;
      const rsrc_t rr = mkrsrc(kk ? kp : vp, SK * 256);
      const int so0 = kk ? w0 + (it + 1) * 4 * STG : u0;
      unsigned xo = kk ? k2o : vvo;
      SCORE(0); ISSUE(0, rr, xo, so0, KSP); SCORE(1); ISSUE(1, rr, xo, so0 + STG, KSP);
      SCORE(2); ISSUE(2, rr, xo, so0 + 2 * STG, KSP); SCORE(3); ISSUE(3, rr, xo, so0 + 3 * STG, KSP);
      sadr += 2048;
      if (it == 1) { if (lane == 0) *(LAS u32x2*)(lds + L_T + w4 * 8) = u32x2{t_a, (unsigned)__builtin_amdgcn_s_memrealtime()}; BUMP(F_C); }
    }
    BUMP(F_K);
    WAITF(F_P, 4, 1);
    f32x4 acc[8];
#pragma unroll
    for (int i = 0; i < 8; ++i) acc[i] = f32x4{0.f, 0.f, 0.f, 0.f};
    unsigned padr = L_P + w4 * 4096 + g * 64 + (c & 3) * 16;
    unsigned dup[4][4] = {};
#define PV(st) do { const u32x4 pf_ = *(LAS u32x4*)(lds + padr + (st) * 256); \
      _Pragma("unroll") for (int w_ = 0; w_ < 4; ++w_) { u32x4 a0_, a1_; \
        _Pragma("unroll") for (int a_ = 0; a_ < 4; ++a_) { a0_[a_] = plo(ba[st][a_][w_], bb[st][a_][w_]); a1_[a_] = phi(ba[st][a_][w_], bb[st][a_][w_]); } \
        acc[2 * w_] = mfma16(a0_, pf_, acc[2 * w_]); acc[2 * w_ + 1] = mfma16(a1_, pf_, acc[2 * w_ + 1]); } \
      SB(); _Pragma("unroll") for (int t_ = 0; t_ < 4; ++t_) asm volatile("" :: "v"(dup[st][t_])); SB(); } while (0)
#pragma unroll 1
    for (int it = 0; it < 3; ++it) {
      const int so0 = u0 + (it + 1) * 4 * STG;
      STEP_V(0, so0); STEP_V(1, so0 + STG); STEP_V(2, so0 + 2 * STG); STEP_V(3, so0 + 3 * STG);
      padr += 1024;
    }
    BUMP(F_T);
    WAITF(F_F, 1, 1);
    const f32x4 fa = *(LAS f32x4*)(lds + L_FA + g * 16);
    {
      const float al = *(LAS float*)(lds + L_HM + w4 * 16 + (c & 3) * 4);
#pragma unroll
      for (int i = 0; i < 8; ++i) acc[i] *= al;
    }
    PV(0); PV(1); PV(2); PV(3);
    if (c < 4) {
#pragma unroll
      for (int i = 0; i < 8; ++i) *(LAS f32x4*)(lds + v_dep + i * 16) = acc[i];
    }
    CFENCE();
    unsigned arr = 0;
    if (lane == 0) arr = LINC(F_MERGE);
    const bool last = __builtin_amdgcn_readfirstlane(arr) == 3;
    CFENCE();
    if (last) {
      f32x4 o0 = {0.f, 0.f, 0.f, 0.f}, o1 = {0.f, 0.f, 0.f, 0.f};
#pragma unroll
      for (int w = 0; w < 4; ++w) { o0 += fa[w] * *(LAS f32x4*)(lds + v_rec + w * 4 * MREC); o1 += fa[w] * *(LAS f32x4*)(lds + v_rec + w * 4 * MREC + 16); }
      u32x4 d0 = {cvtpk(o0[0], o1[0]), tag, cvtpk(o0[1], o1[1]), tag}, d1 = {cvtpk(o0[2], o1[2]), tag, cvtpk(o0[3], o1[3]), tag};
      asm volatile("" : "+v"(d0), "+v"(d1));
      __builtin_amdgcn_raw_buffer_store_b128(d0, rws, v_ost, s_ost, AUX_SC1);
      __builtin_amdgcn_raw_buffer_store_b128(d1, rws, v_ost + 128, s_ost, AUX_SC1);
      asm volatile("s_nop 1" ::: "memory");
    }
  } else {
    u32x4 ba[4][4], bb[4][4];
    ISSUE(0, rk, k2o, w0, KSP);
    __builtin_amdgcn_s_barrier(); CFENCE();
    SB();
    ISSUE(1, rk, k2o, w0 + STG, KSP); ISSUE(2, rk, k2o, w0 + 2 * STG, KSP); ISSUE(3, rk, k2o, w0 + 3 * STG, KSP);
    WAITF(F_QP, 4, 1);
    tag = RFL(flg[F_TAG]);
    u32x4 qg[2];
#pragma unroll 1
    for (unsigned spin = 0; spin < SPIN_CAP; ++spin) {
      CFENCE();
      qg[0] = __builtin_amdgcn_raw_buffer_load_b128(rws, lane * 16, WS_QG + (kvh * 8 + b) * QS + w4 * 2048, AUX_SC1);
      qg[1] = __builtin_amdgcn_raw_buffer_load_b128(rws, lane * 16 + 1024, WS_QG + (kvh * 8 + b) * QS + w4 * 2048, AUX_SC1);
      if (__all(ok21(qg[0], tag) & ok21(qg[1], tag))) break;
    }
    {
      const float a0 = val21(qg[0][0]) + val21(qg[1][0]), a1 = val21(qg[0][1]) + val21(qg[1][1]), b0 = val21(qg[0][2]) + val21(qg[1][2]), b1 = val21(qg[0][3]) + val21(qg[1][3]);
      auto t0 = __builtin_amdgcn_permlane32_swap(__float_as_uint(a0), __float_as_uint(b0), false, false);
      auto t1 = __builtin_amdgcn_permlane32_swap(__float_as_uint(a1), __float_as_uint(b1), false, false);
      const int p = 2 * c + (lane >> 5);
      *(LAS unsigned*)(lds + L_Q + w4 * 256 + (g & 1) * 128 + (p >> 3) * 32 + (p & 3) * 8 + ((p >> 2) & 1) * 4) =
          cvtpk(__uint_as_float(t0[0]) + __uint_as_float(t0[1]), __uint_as_float(t1[0]) + __uint_as_float(t1[1]));
    }
    BUMP(F_Q);
    TAIL_ADDR_AND_Q();
    SCORE(0); SCORE(1); SCORE(2); SCORE(3);
    WAITF(F_C, 4, 8);
    if (wave == 1) {
      const u32x2 tk = *(LAS u32x2*)(lds + L_T + (c & 3) * 8), tz = *(LAS u32x2*)(lds + L_T);
      int db = (int)(tk[1] - tz[1]), dl = (int)(tk[1] - tk[0]);
      db += __builtin_amdgcn_mov_dpp(db, XOR1, 0xf, 0xf, true); db += __builtin_amdgcn_mov_dpp(db, XOR2, 0xf, 0xf, true);
      dl += __builtin_amdgcn_mov_dpp(dl, XOR1, 0xf, 0xf, true); dl += __builtin_amdgcn_mov_dpp(dl, XOR2, 0xf, 0xf, true);
      if (lane < 8 && sampled) __builtin_amdgcn_raw_buffer_store_b128(u32x4{tz[1] + (unsigned)(db >> 2), tag, (unsigned)dl, tag}, rws, vvo, WS_TG + sidx * 128, AUX_SC1);
      asm volatile("s_nop 1" ::: "memory");
    }
#define PROBS(lo, n) do { f32x2 sc_[n]; \
      _Pragma("unroll") for (int sg = 0; sg < (n); ++sg) sc_[sg] = *(LAS f32x2*)(lds + L_S + w4 * 8192 + ((lo) + sg) * 512 + v_l8); \
      _Pragma("unroll") for (int sg = 0; sg < (n); ++sg) mx = fmaxf(mx, fmaxf(sc_[sg][0], sc_[sg][1])); \
      mx = fmaxf(mx, dpp<ROR4>(mx)); mx = fmaxf(mx, dpp<ROR8>(mx)); mx = xrow_max(mx); \
      mc = mx * C2; l0 = 0.f; l1 = 0.f; \
      _Pragma("unroll") for (int sg = 0; sg < (n); ++sg) { \
        const float p0 = __builtin_amdgcn_exp2f(fmaf(sc_[sg][0], C2, -mc)), p1 = __builtin_amdgcn_exp2f(fmaf(sc_[sg][1], C2, -mc)); \
        l0 += p0; l1 += p1; \
        *(LAS unsigned*)(lds + L_P + w4 * 4096 + ((lo) + sg) * 256 + g * 64 + (c & 3) * 16 + tau * 4) = cvtpk(p0, p1); } } while (0)
    float mx = -INFINITY, mc, l0, l1;
    PROBS(0, 12);
    BUMP(F_P);
    const float mca = mc, la = l0 + l1;
    if (wave == 2) {
#pragma unroll 1
      for (unsigned spin = 0; spin < 3; ++spin) {
        __builtin_amdgcn_s_sleep(LXS);
        CFENCE();
        u32x4 ck = {0u, ~tag, 0u, ~tag};
        if (lane < 16) ck = __builtin_amdgcn_raw_buffer_load_b128(rws, lane * 128, WS_TG, AUX_SC1);
        const bool have = (ck[1] == tag) & (ck[3] == tag);
        if (__builtin_popcountll(__ballot(have)) > 8) {
          const unsigned now = (unsigned)__builtin_amdgcn_s_memrealtime();
          const int ka = have ? (int)(ck[0] - now) + 8192 : 0x7fffffff, kl = have ? (int)ck[2] : 0x7fffffff;
          int ma = 0, ml = 0;
#pragma unroll 1
          for (int bit = 4096; bit > 0; bit >>= 1) {
            if (__builtin_popcountll(__ballot(ka < ma + bit)) < 8) ma += bit;
            if (__builtin_popcountll(__ballot(kl < ml + bit)) < 8) ml += bit;
          }
          const unsigned r1 = ((unsigned)ml * (PACE / 2)) >> 10;
          if (lane == 0) *(volatile LAS u32x2*)(lds + L_F + 4 * F_REF) = u32x2{now - 8192u + (unsigned)ma + ((16 * r1) >> 6) + MARGIN, r1};
          break;
        }
      }
    }
    WAITF(F_K, 4, 8);
    PROBS(12, 4);
    {
      const float al = __builtin_amdgcn_exp2f(mca - mc);
      float l = fmaf(la, al, l0 + l1);
      l += dpp<ROR8>(l); l += dpp<ROR4>(l); l = xrow_sum(l);
      if (lane < 4) { *(LAS f32x2*)(lds + L_ML + lane * 32 + w4 * 8) = f32x2{mc, l}; *(LAS float*)(lds + L_HM + w4 * 16 + lane * 4) = al; }
      BUMP(F_L);
    }
    if (wave == 0 || wave == 3) {
      WAITF(F_L, 4, 4);
      const f32x2 ml = *(LAS f32x2*)(lds + L_ML + c * 8);
      const unsigned lbase = WS_LG + (b * 8 + kvh) * 512;
      if (wave == 0) {
        float M = fmaxf(ml[0], dpp<XOR1>(ml[0])); M = fmaxf(M, dpp<XOR2>(M));
        float L = ml[1] * __builtin_amdgcn_exp2f(ml[0] - M); L += dpp<XOR1>(L); L += dpp<XOR2>(L);
        if (lane < 16) __builtin_amdgcn_raw_buffer_store_b64(u32x2{__float_as_uint((c & 1) ? L : M), tag}, rws, lane * 8, lbase + s * 128, AUX_SC1);
      } else {
        u32x4 lg;
        __builtin_amdgcn_s_sleep(127); __builtin_amdgcn_s_sleep(60);
#pragma unroll 1
        for (unsigned spin = 0; spin < (SPIN_CAP >> 3); ++spin) {
          __builtin_amdgcn_s_sleep(32);
          CFENCE();
          lg = u32x4{0u, tag, 0u, tag};
          if (lane < 16) lg = __builtin_amdgcn_raw_buffer_load_b128(rws, (c & 3) * 128 + (c >> 2) * 32, lbase, AUX_SC1);
          if (__all((lg[1] == tag) & (lg[3] == tag))) break;
        }
        const float ms = __uint_as_float(lg[0]);
        float G = fmaxf(ms, dpp<XOR1>(ms)); G = fmaxf(G, dpp<XOR2>(G));
        float E = __uint_as_float(lg[2]) * __builtin_amdgcn_exp2f(ms - G); E += dpp<XOR1>(E); E += dpp<XOR2>(E);
        if (lane < 16) *(LAS float*)(lds + L_FA + lane * 4) = __builtin_amdgcn_exp2f(ml[0] - G) / E;
        BUMP(F_F);
      }
    }
    const rsrc_t rw = mkrsrc(wo + (size_t)H * 131072 + b * 128, 131072);
    unsigned v_wld = (32 * (g & 1) + 16 * (g >> 1)) * 1024 + c * 8, s_wld = RFL((64 * (w4 >> 1) + 4 * (w4 & 1)) * 1024);
    asm volatile("" : "+v"(v_wld), "+s"(s_wld));
    WAITF(F_T, 1, 16);
    WAITF(F_T, 4, 1);
    {
      const unsigned r0 = RFL(flg[F_REF]), r1 = RFL(flg[F_REF + 1]), tw = r0 + ((48 * r1) >> 6) + WOFF;
#pragma unroll 1
      for (unsigned spin = 0; spin < 64; ++spin) { if (flg[F_MERGE] >= 4u || (r1 != 0u && (int)((unsigned)__builtin_amdgcn_s_memrealtime() - tw) >= 0)) break; __builtin_amdgcn_s_sleep(1); }
    }
    u32x2 wr[8];
#pragma unroll
    for (int i = 0; i < 8; ++i) wr[i] = __builtin_amdgcn_raw_buffer_load_b64(rw, v_wld + (2 * (i >> 2) + (i & 1)) * 1024, s_wld + 8 * ((i >> 1) & 1) * 1024, 0);
#pragma unroll
    for (int w = 0; w < 2; ++w) {
      u32x4 f0, f1;
#pragma unroll
      for (int a = 0; a < 4; ++a) { f0[a] = plo(wr[2 * a][w], wr[2 * a + 1][w]); f1[a] = phi(wr[2 * a][w], wr[2 * a + 1][w]); }
      *(LAS u32x4*)(lds + L_W + (w4 * 4 + 2 * w) * 1024 + vvo) = f0; *(LAS u32x4*)(lds + L_W + (w4 * 4 + 2 * w + 1) * 1024 + vvo) = f1;
    }
    BUMP(F_W);
  }

  tag = RFL(tag); s_old = RFL(s_old); s_yst = RFL(s_yst); s_yld = RFL(s_yld); s_out = RFL(s_out);
  WAITF(F_MERGE, 4, 1);
  WAITF(F_W, 4, 1);
  u32x4 wf[16], og0, og1;
#pragma unroll
  for (int i = 0; i < 16; ++i) wf[i] = *(LAS u32x4*)(lds + L_W + i * 1024 + vvo);
  {
    bool no = true;
#pragma unroll 1
    for (unsigned spin = 0; spin < SPIN_CAP; ++spin) {
      CFENCE();
      if (no) {
        og0 = __builtin_amdgcn_raw_buffer_load_b128(rws, k2o, s_old, AUX_SC1);
        og1 = __builtin_amdgcn_raw_buffer_load_b128(rws, k2o, s_old + OS, AUX_SC1);
        no = !((og0[1] == tag) & (og0[3] == tag) & (og1[1] == tag) & (og1[3] == tag));
      }
      if (!__any(no)) break;
      __builtin_amdgcn_s_sleep(PSLEEP);
    }
  }
  {
    const int u = c & 3;
    const u32x4 a0 = {u == 0 ? og0[0] : 0u, u == 0 ? og0[2] : 0u, u == 1 ? og0[0] : 0u, u == 1 ? og0[2] : 0u};
    const u32x4 a1 = {u == 2 ? og0[0] : 0u, u == 2 ? og0[2] : 0u, u == 3 ? og0[0] : 0u, u == 3 ? og0[2] : 0u};
    const u32x4 a2 = {u == 0 ? og1[0] : 0u, u == 0 ? og1[2] : 0u, u == 1 ? og1[0] : 0u, u == 1 ? og1[2] : 0u};
    const u32x4 a3 = {u == 2 ? og1[0] : 0u, u == 2 ? og1[2] : 0u, u == 3 ? og1[0] : 0u, u == 3 ? og1[2] : 0u};
    f32x4 d[4];
#pragma unroll
    for (int i = 0; i < 4; ++i) d[i] = mfma16(a0, wf[i], f32x4{0.f, 0.f, 0.f, 0.f});
#pragma unroll
    for (int i = 0; i < 4; ++i) d[i] = mfma16(a1, wf[4 + i], d[i]);
#pragma unroll
    for (int i = 0; i < 4; ++i) d[i] = mfma16(a2, wf[8 + i], d[i]);
#pragma unroll
    for (int i = 0; i < 4; ++i) d[i] = mfma16(a3, wf[12 + i], d[i]);
    float y[4];
#pragma unroll
    for (int i = 0; i < 4; ++i) y[i] = (d[i][0] + d[i][1]) + (d[i][2] + d[i][3]);
#pragma unroll
    for (int i = 0; i < 2; ++i) { auto t = __builtin_amdgcn_permlane32_swap(__float_as_uint(y[i]), __float_as_uint(y[i + 2]), false, false); y[i] = __uint_as_float(t[0]) + __uint_as_float(t[1]); }
#pragma unroll
    for (int i = 0; i < 2; ++i) { auto t = __builtin_amdgcn_permlane16_swap(__float_as_uint(y[i]), __float_as_uint(y[i]), false, false); y[i] = __uint_as_float(t[0]) + __uint_as_float(t[1]); }
    if ((g & 1) == 0) __builtin_amdgcn_raw_buffer_store_b64(pack21(y[0], y[1], tag + 1u), rws, v_yst, s_yst, AUX_SC1);
  }
  if (wave == b && j < 16) {
    __builtin_amdgcn_s_sleep(RDLY);
    u32x4 yg[4];
    bool ny[4] = {true, true, true, true};
#pragma unroll 1
    for (unsigned spin = 0; spin < SPIN_CAP; ++spin) {
      CFENCE();
#pragma unroll
      for (int n = 0; n < 4; ++n) if (ny[n]) yg[n] = __builtin_amdgcn_raw_buffer_load_b128(rws, v_yld, s_yld + n * 16 * QS, AUX_SC1);
#pragma unroll
      for (int n = 0; n < 4; ++n) ny[n] = !ok21(yg[n], tag + 1u);
      if (!__any(ny[0] | ny[1] | ny[2] | ny[3])) break;
      __builtin_amdgcn_s_sleep(PSLEEP2);
    }
    float r[4];
#pragma unroll
    for (int i = 0; i < 4; ++i) r[i] = (val21(yg[0][i]) + val21(yg[1][i])) + (val21(yg[2][i]) + val21(yg[3][i]));
#pragma unroll
    for (int i = 0; i < 2; ++i) {
      auto t = __builtin_amdgcn_permlane32_swap(__float_as_uint(r[i]), __float_as_uint(r[i + 2]), false, false);
      r[i] = __uint_as_float(t[0]) + __uint_as_float(t[1]);
      auto z = __builtin_amdgcn_permlane16_swap(__float_as_uint(r[i]), __float_as_uint(r[i]), false, false);
      r[i] = __uint_as_float(z[0]) + __uint_as_float(z[1]);
      r[i] += dpp<ROR8>(r[i]);
    }
    if ((lane & 24) == 0) __builtin_amdgcn_raw_buffer_store_b32(cvtpk(r[0], r[1]), mkrsrc(out, 8192), v_out, s_out, 0);
    if (lane == 0 && blk == 0) __builtin_amdgcn_raw_buffer_store_b32(tag, rws, 0, WS_EPOCH, AUX_SC1);
  }
}

}

extern "C" void kernel_launch(void* const* d_in, const int* in_sizes, int n_in, void* d_out, int out_size, void* d_ws, size_t ws_size, hipStream_t stream) {
  fused_gqa<<<256, NTHR, 0, stream>>>((const unsigned char*)d_in[0], (const unsigned char*)d_in[1], (const unsigned char*)d_in[2], (const unsigned char*)d_in[3],
                                      (const unsigned char*)d_in[4], (unsigned char*)d_out, (unsigned char*)d_ws);
}
```
